```python
import torch

def _softmax(x, axis=-1):
    return torch.softmax(x, dim=axis)

def _gelu(x):
    return torch.nn.functional.gelu(x, approximate='tanh')

def _silu(x):
    return torch.nn.functional.silu(x)

def _sigmoid(x):
    return torch.sigmoid(x)

def _layernorm(x, axis=-1, eps=1e-05):
    m = x.mean(dim=axis, keepdim=True)
    v = x.var(dim=axis, keepdim=True, unbiased=False)
    return (x - m) / torch.sqrt(v + eps)

def _scatter_add(tgt, idx, src, axis=0):
    return tgt.clone().index_add_(axis, idx, src)

def _segment_sum(src, seg, n):
    out = torch.zeros((n,) + src.shape[1:], dtype=src.dtype)
    return out.index_add_(0, seg, src)

def _pad(x, pw):
    flat = tuple((v for pair in reversed(pw) for v in pair))
    return torch.nn.functional.pad(x, flat)
kernel_name = 'recurrent_scan__memory_aug_recurrent_mem_tr__glu_gated_bilinear__prompt'

def setup_inputs():

    _w = lambda a: (lambda t: t.to(torch.bfloat16) if t.is_floating_point() else t)(torch.as_tensor(a))

    def _A_setup():
        B, S, Dm = (1, 2048, 512)
        H, Dh = (32, 128)
        M, seg = (8, 32)
        seg = min(seg, S)
        S = S // seg * seg
        return {'x': _w((lambda *s: torch.randn(*s, dtype=torch.bfloat16) * 0.1)(B, S, Dm)), 'mem0': _w((lambda *s: torch.randn(*s, dtype=torch.bfloat16) * 1.6)(B, M, Dm)), 'Wq': _w((lambda *s: torch.randn(*s, dtype=torch.bfloat16) * 0.8)(Dm, H, Dh) * Dm ** (-0.5)), 'Wk': _w((lambda *s: torch.randn(*s, dtype=torch.bfloat16) * 0.8)(Dm, H, Dh) * Dm ** (-0.5)), 'Wv': _w((lambda *s: torch.randn(*s, dtype=torch.bfloat16) * 1.6)(Dm, H, Dh) * Dm ** (-0.5)), 'Wo': _w((lambda *s: torch.randn(*s, dtype=torch.bfloat16) * 1.6)(H, Dh, Dm) * (H * Dh) ** (-0.5)), 'seg_len': seg}
    _A_in = _A_setup()

    def _B_setup():
        B, S, D, F = (1, 2048, 512, 1376)
        return {'x': _w((lambda *s: torch.randn(*s, dtype=torch.bfloat16) * 0.1)(B, S, D)), 'w_gate': _w((lambda *s: torch.randn(*s, dtype=torch.bfloat16) * 0.4)(D, F) * D ** (-0.5)), 'w_up': _w((lambda *s: torch.randn(*s, dtype=torch.bfloat16) * 0.4)(D, F) * D ** (-0.5)), 'w_down': _w((lambda *s: torch.randn(*s, dtype=torch.bfloat16) * 0.4)(F, D) * F ** (-0.5))}
    _B_in = _B_setup()
    return {'A_x': _A_in['x'], 'A_mem0': _A_in['mem0'], 'A_Wq': _A_in['Wq'], 'A_Wk': _A_in['Wk'], 'A_Wv': _A_in['Wv'], 'A_Wo': _A_in['Wo'], 'A_seg_len': _A_in['seg_len'], 'B_x': _B_in['x'], 'B_w_gate': _B_in['w_gate'], 'B_w_up': _B_in['w_up'], 'B_w_down': _B_in['w_down']}

def reference(A_x, A_mem0, A_Wq, A_Wk, A_Wv, A_Wo, A_seg_len, B_x, B_w_gate, B_w_up, B_w_down):

    def _A_ref(x, mem0, Wq, Wk, Wv, Wo, seg_len):
        B, S, Dm = x.shape
        H, Dh = (Wq.shape[1], Wq.shape[2])
        M = mem0.shape[1]
        scale = Dh ** (-0.5)
        n_seg = S // seg_len
        L = M + seg_len
        idx = torch.arange(L)
        mask = (idx[:, None] < M) | (idx[:, None] >= idx[None, :])
        mem = mem0
        outs = []
        for s in range(n_seg):
            xs = x[:, s * seg_len:(s + 1) * seg_len, :].clone()
            h = torch.concatenate([mem, xs], axis=1)
            q = torch.einsum('btd,dhe->bhte', h, Wq)
            k = torch.einsum('btd,dhe->bhte', h, Wk)
            v = torch.einsum('btd,dhe->bhte', h, Wv)
            scores = torch.einsum('bhqe,bhke->bhqk', q, k) * scale
            scores = torch.where(mask[None, None], scores, -1000000000.0)
            p = _softmax(scores, axis=-1)
            o = torch.einsum('bhqk,bhke->bhqe', p, v)
            o = torch.einsum('bhte,hed->btd', o, Wo)
            mem = o[:, :M, :].clone()
            outs.append(o[:, M:, :])
        return torch.concatenate(outs, axis=1)

    def _B_ref(x, w_gate, w_up, w_down):
        g = torch.einsum('bsd,df->bsf', x, w_gate)
        u = torch.einsum('bsd,df->bsf', x, w_up)
        h = g * u
        return torch.einsum('bsf,fd->bsd', h, w_down)
    x = A_x
    xn = _layernorm(x, axis=-1)
    s = _A_ref(xn, A_mem0, A_Wq, A_Wk, A_Wv, A_Wo, A_seg_len)
    g = _silu(_B_ref(xn, B_w_gate, B_w_up, B_w_down))
    return x + s * g
```

```cpp
#include <hip/hip_runtime.h>
#include <hip/hip_bf16.h>
#include <stdint.h>
#include <type_traits>

#define DI __device__ __forceinline__
#define GAS __attribute__((address_space(1)))
#define LAS __attribute__((address_space(3)))
typedef __bf16 bf8 __attribute__((ext_vector_type(8)));
typedef __bf16 bf2 __attribute__((ext_vector_type(2)));
typedef short s4v __attribute__((ext_vector_type(4)));
typedef float f4 __attribute__((ext_vector_type(4)));
typedef float f2 __attribute__((ext_vector_type(2)));
typedef unsigned u4 __attribute__((ext_vector_type(4)));
typedef unsigned u2 __attribute__((ext_vector_type(2)));
typedef unsigned long long u64;

constexpr size_t MB1 = 1u << 20;
constexpr size_t WS_FLAGS = 3 * MB1 + 0x40000;
constexpr size_t F_EPOCH = 0, F_PAR = 0x100, F_POI = 0x200, F_LN = 0x800, F_LOSE = 0x1000, F_Q = 0x2000, F_H = 0x4000, F_SLOT = 0x8000, F_PROG = 0xC000, F_KV = 0x10000, F_X = 0x20000;
constexpr size_t WS_XF = 4 * MB1, WS_HF = 6 * MB1, WS_KVM = 12 * MB1, WS_OF = 28 * MB1, WS_KV = 44 * MB1, WS_DBG = 76 * MB1, WS_PX = 80 * MB1, WS_QF = 112 * MB1, WS_OFL = 128 * MB1;
constexpr size_t WS_TS = 3 * MB1 + 0x80000;
constexpr unsigned SPIN_CAP = 1u << 15;
#define SLOT_XMASK 7u
#define QF_DELAY 2
#define FF_DELAY 0
#define TAIL_NAP 45
#define PX_NAP 8
constexpr int LDS_BYTES = 160 * 1024;

struct Par {
    const void *wq, *wk, *wv, *wo, *mem0; void *kv, *kvf, *kvm, *prog, *ws, *par, *dbg; int W, PI, RB, LA;
    const void *x, *wg, *wu, *wd; void* out; int phases, pad, CB, CA;
};

DI unsigned rfl(unsigned v) { return __builtin_amdgcn_readfirstlane(v); }
DI f4 mfma(bf8 a, bf8 b, f4 c) { return __builtin_amdgcn_mfma_f32_16x16x32_bf16(a, b, c, 0, 0, 0); }
DI unsigned pk2(float lo, float hi) { f2 v = {lo, hi}; return __builtin_bit_cast(unsigned, __builtin_convertvector(v, bf2)); }
DI float rbf(float v) { return (float)(__bf16)v; }
DI float bflo(unsigned v) { return __builtin_bit_cast(float, v << 16); }
DI float bfhi(unsigned v) { return __builtin_bit_cast(float, v & 0xffff0000u); }
DI bf8 asbf(u4 v) { return __builtin_bit_cast(bf8, v); }
DI __amdgpu_buffer_rsrc_t rsrc(const void* p, unsigned bytes) { return __builtin_amdgcn_make_buffer_rsrc((void*)p, 0, bytes, 0x00020000); }
DI u4 ld16(__amdgpu_buffer_rsrc_t r, unsigned voff, unsigned soff) { return __builtin_amdgcn_raw_buffer_load_b128(r, voff, soff, 16); }
DI void st16(__amdgpu_buffer_rsrc_t r, u4 v, unsigned voff, unsigned soff) {
    __builtin_amdgcn_raw_buffer_store_b128(v, r, voff, soff, 16);
    asm volatile("s_nop 1" :: "v"(v) : "memory");
}
DI void drain() { asm volatile("s_waitcnt vmcnt(0)" ::: "memory"); }
DI void lds_barrier() { asm volatile("s_waitcnt lgkmcnt(0)\n\ts_barrier" ::: "memory"); }
template <bool SC1> DI void glds(const void* gbase, unsigned voff, unsigned lds) {
    unsigned keep;
    if (SC1) asm volatile("s_mov_b32 %0, m0\n\ts_mov_b32 m0, %3\n\ts_nop 0\n\tglobal_load_lds_dwordx4 %1, %2 sc1\n\ts_mov_b32 m0, %0" : "=&s"(keep) : "v"(voff), "s"(gbase), "s"(lds) : "memory");
    else asm volatile("s_mov_b32 %0, m0\n\ts_mov_b32 m0, %3\n\ts_nop 0\n\tglobal_load_lds_dwordx4 %1, %2\n\ts_mov_b32 m0, %0" : "=&s"(keep) : "v"(voff), "s"(gbase), "s"(lds) : "memory");
}
template <bool SC1, int LOFF> DI void gldsi(const void* gbase, unsigned voff, unsigned lds) {
    unsigned keep;
    if (SC1) asm volatile("s_mov_b32 %0, m0\n\ts_add_u32 m0, %3, %4\n\ts_nop 0\n\tglobal_load_lds_dwordx4 %1, %2 sc1\n\ts_mov_b32 m0, %0" : "=&s"(keep) : "v"(voff), "s"(gbase), "s"(lds), "i"(LOFF) : "memory", "scc");
    else asm volatile("s_mov_b32 %0, m0\n\ts_add_u32 m0, %3, %4\n\ts_nop 0\n\tglobal_load_lds_dwordx4 %1, %2\n\ts_mov_b32 m0, %0" : "=&s"(keep) : "v"(voff), "s"(gbase), "s"(lds), "i"(LOFF) : "memory", "scc");
}
template <int LOFF> DI void gldsn(const void* gbase, unsigned voff, unsigned lds) {
    asm volatile("s_add_u32 m0, %2, %3\n\ts_nop 0\n\tglobal_load_lds_dwordx4 %0, %1 sc1" :: "v"(voff), "s"(gbase), "s"(lds), "i"(LOFF) : "memory", "scc");
}
template <int J> DI void glds4(const void* gbase, unsigned voff, unsigned lds) {
    if (J % 4 == 0) asm volatile("s_add_u32 m0, %2, %3\n\ts_nop 0\n\tglobal_load_lds_dwordx4 %0, %1 sc1" :: "v"(voff), "s"(gbase), "s"(lds), "i"(J * 1024) : "memory", "scc");
    else asm volatile("global_load_lds_dwordx4 %0, %1 offset:%2 sc1" :: "v"(voff), "s"(gbase), "i"(J % 4 * 1024) : "memory");
}
template <int I, int N, class F> DI void sfor(F&& f) { if constexpr (I < N) { f(std::integral_constant<int, I>{}); sfor<I + 1, N>(f); } }
DI u2 trd(unsigned a) { return __builtin_bit_cast(u2, __builtin_amdgcn_ds_read_tr16_b64_v4i16((LAS s4v*)a)); }
DI bf8 trfrag(unsigned a0, unsigned a1) { u2 x = trd(a0), y = trd(a1); return asbf((u4){x.x, x.y, y.x, y.y}); }
DI u4 lds16(unsigned a) { return *(LAS u4*)a; }
DI u64 ldflag(const void* p) { return __hip_atomic_load((GAS u64*)p, __ATOMIC_RELAXED, __HIP_MEMORY_SCOPE_AGENT); }
DI int count_of(u64 v, u64 tg) { const u64 d = v - tg; return d < 128 ? (int)d : -1; }
template <int SLEEP = 4> DI void wait_set(const void* p, bool need, u64 tg) {
    for (unsigned s = 0; s < SPIN_CAP; ++s) {
        const u64 v = ldflag(p);
        if (__all(v == tg || !need)) break;
        __builtin_amdgcn_s_sleep(SLEEP);
    }
    asm volatile("" ::: "memory");
}
DI void wait_prog(const void* p, int want, u64 tg) {
    for (unsigned s = 0; s < SPIN_CAP; ++s) {
        const int mine = count_of(ldflag(p), tg);
        const int c0 = __builtin_amdgcn_readlane(mine, 0), c1 = __builtin_amdgcn_readlane(mine, 1), cur = c0 < c1 ? c0 : c1;
        if (cur >= want) break;
        if (want - cur > 1) __builtin_amdgcn_s_sleep(48); else __builtin_amdgcn_s_sleep(3);
    }
    asm volatile("" ::: "memory");
}
template <int SLEEP> DI void doze_until(const void* p, int want, u64 tg) {
    for (unsigned s = 0; s < SPIN_CAP && (int)rfl(count_of(ldflag(p), tg)) < want; ++s) __builtin_amdgcn_s_sleep(SLEEP);
}
DI void* uni(const void* p) { const uintptr_t a = (uintptr_t)p; return (void*)(((uintptr_t)rfl((unsigned)(a >> 32)) << 32) | rfl((unsigned)a)); }
DI void setflag(void* q, u64 v) {
    void* p = uni(q);
    u64 keep;
    asm volatile("s_mov_b64 %0, exec\n\ts_mov_b64 exec, 1\n\tglobal_store_dwordx2 %1, %2, %3 sc1\n\ts_mov_b64 exec, %0" : "=&s"(keep) : "v"(0u), "v"(v), "s"(p) : "memory");
}

template <int CTRL> DI float dpp_add(float v) {
    return v + __builtin_bit_cast(float, __builtin_amdgcn_update_dpp(0, __builtin_bit_cast(int, v), CTRL, 0xf, 0xf, true));
}
DI float sum8(float v) { v = dpp_add<0xB1>(v); v = dpp_add<0x4E>(v); return dpp_add<0x141>(v); }
struct LnRows { u4 v[8]; };
DI void ln_issue(const Par& p, int seg, int wave, int lane, LnRows& r) {
    const char* x = (const char*)p.x + (size_t)seg * 32768 + wave * 8192;
    const unsigned off = (lane >> 3) * 1024 + (lane & 7) * 16;
    asm volatile("global_load_dwordx4 %0, %8, %9\n\tglobal_load_dwordx4 %1, %8, %9 offset:128\n\tglobal_load_dwordx4 %2, %8, %9 offset:256\n\tglobal_load_dwordx4 %3, %8, %9 offset:384\n\t"
                 "global_load_dwordx4 %4, %8, %9 offset:512\n\tglobal_load_dwordx4 %5, %8, %9 offset:640\n\tglobal_load_dwordx4 %6, %8, %9 offset:768\n\tglobal_load_dwordx4 %7, %8, %9 offset:896"
                 : "=&v"(r.v[0]), "=&v"(r.v[1]), "=&v"(r.v[2]), "=&v"(r.v[3]), "=&v"(r.v[4]), "=&v"(r.v[5]), "=&v"(r.v[6]), "=&v"(r.v[7]) : "v"(off), "s"(x) : "memory");
}
template <int YOUNGER> DI void ln_finish(int wave, int lane, LnRows& r, unsigned lb) {
    asm volatile("s_waitcnt vmcnt(%8)" : "+v"(r.v[0]), "+v"(r.v[1]), "+v"(r.v[2]), "+v"(r.v[3]), "+v"(r.v[4]), "+v"(r.v[5]), "+v"(r.v[6]), "+v"(r.v[7]) : "i"(YOUNGER) : "memory");
    const int t = wave * 8 + (lane >> 3), part = lane & 7;
    float s = 0.f;
#pragma unroll
    for (int i = 0; i < 8; ++i)
#pragma unroll
        for (int j = 0; j < 4; ++j) s += bflo(r.v[i][j]) + bfhi(r.v[i][j]);
    const float mean = sum8(s) * (1.f / 512.f);
    float q = 0.f;
#pragma unroll
    for (int i = 0; i < 8; ++i)
#pragma unroll
        for (int j = 0; j < 4; ++j) { const float a = bflo(r.v[i][j]) - mean, b = bfhi(r.v[i][j]) - mean; q += a * a + b * b; }
    const float mb = rbf(mean), inv = 1.f / rbf(sqrtf(rbf(rbf(sum8(q) * (1.f / 512.f)) + 1e-5f)));
#pragma unroll
    for (int i = 0; i < 8; ++i) {
        u4 o;
#pragma unroll
        for (int j = 0; j < 4; ++j) o[j] = pk2(rbf(bflo(r.v[i][j]) - mb) * inv, rbf(bfhi(r.v[i][j]) - mb) * inv);
        const int ch = part + 8 * i;
        *(LAS u4*)(lb + ((t >> 4) * 16 + (ch >> 2)) * 1024 + ((ch & 3) * 16 + (t & 15)) * 16) = o;
    }
}
DI void phase_ln(const Par& p, u64 tg, int seg, int wave, int lane) {
    char* ws = (char*)p.ws;
    LnRows rows;
    ln_issue(p, seg, wave, lane, rows);
    ln_finish<0>(wave, lane, rows, 0);
    lds_barrier();
    const __amdgpu_buffer_rsrc_t r = rsrc(ws + WS_XF, 2 * MB1);
#pragma unroll
    for (int i = 0; i < 8; ++i) st16(r, lds16((wave * 8 + i) * 1024 + lane * 16), lane * 16, seg * 32768 + (wave * 8 + i) * 1024);
    drain();
    lds_barrier();
    if (wave == 0) setflag(ws + WS_FLAGS + F_LN + seg * 8, tg);
}

constexpr unsigned XB0 = 98304;
DI void xn_dma(const Par& p, int seg, int buf, int wave, int lane) {
    const char* xf = (const char*)p.ws + WS_XF + (size_t)seg * 32768 + wave * 8192;
    const unsigned lb = (buf ? XB0 : 0u) + wave * 8192;
    sfor<0, 8>([&](auto I) { gldsi<true, I * 1024>(xf, lane * 16 + I * 1024, lb); });
}

template <bool PAIR0, bool PAIR1, bool OWNLN>
DI void load_w2(const Par& p, u64 tg, int seg0, int seg1, bf8 (&W)[4][16], f4 (&acc)[4][2], const char* m0, const char* m1, unsigned rs, int nch, int wave, int lane) {
    const int lo = lane & 15, g = lane >> 4, q = lo >> 2, pp = lo & 3;
    unsigned src[2];
#pragma unroll
    for (int b = 0; b < 2; ++b) {
        const int ch = lo ^ (2 * (g | (b << 2)));
        src[b] = g * rs + (ch < nch ? ch : 0) * 16;
    }
    auto slot = [](int c) { return (c & 3) == 3 ? 131072u : (c & 3) * 32768u; };
    auto issue = [&](int c) {
        const unsigned lb = slot(c) + wave * 4096;
        sfor<0, 4>([&](auto N) {
            const unsigned vo = src[(N >> 1) & 1] + (c * 64 + wave * 16 + N * 4) * rs;
            gldsi<false, N * 1024>(m0, vo, lb);
            gldsi<false, 16384 + N * 1024>(m1, vo, lb);
        });
    };
    const int f = 2 * (q | ((g & 1) << 2));
    unsigned ad[2][2];
#pragma unroll
    for (int a = 0; a < 2; ++a) {
        ad[0][a] = (8 * g + q) * 256 + (PAIR0 ? (((4 * wave + pp) ^ f) * 16 + a * 8) : (((4 * wave + 2 * a + (pp >> 1)) ^ f) * 16 + (pp & 1) * 8));
        ad[1][a] = (8 * g + q) * 256 + 16384 + (PAIR1 ? (((4 * wave + pp) ^ f) * 16 + a * 8) : (((4 * wave + 2 * a + (pp >> 1)) ^ f) * 16 + (pp & 1) * 8));
    }
    u64 lnv = 0;
    if (OWNLN) {
        LnRows rows;
        ln_issue(p, seg0, wave, lane, rows);
        issue(0); issue(1); issue(2);
        ln_finish<24>(wave, lane, rows, XB0);
    } else { issue(0); issue(1); issue(2); }
#pragma unroll
    for (int c = 0; c < 8; ++c) {
        if (c <= 4) asm volatile("s_waitcnt vmcnt(16)" ::: "memory");
        else if (c == 5) asm volatile("s_waitcnt vmcnt(17)" ::: "memory");
        else asm volatile("s_waitcnt vmcnt(8)" : "+v"(lnv) :: "memory");
        lds_barrier();
        if (c == 4) asm volatile("global_load_dwordx2 %0, %1, %2 sc1" : "=v"(lnv) : "v"(0u), "s"((char*)p.ws + WS_FLAGS + F_LN + seg1 * 8) : "memory");
        if (c + 3 < 8) issue(c + 3);
        if (c == 6) {
            if (!__all(lnv == tg)) wait_set((char*)p.ws + WS_FLAGS + F_LN + seg1 * 8, true, tg);
            xn_dma(p, seg1, 0, wave, lane);
        }
#pragma unroll
        for (int k2 = 0; k2 < 2; ++k2)
#pragma unroll
            for (int m = 0; m < 2; ++m)
#pragma unroll
                for (int a = 0; a < 2; ++a) {
                    const unsigned b = ad[m][a] + slot(c) + k2 * 8192;
                    W[2 * m + a][2 * c + k2] = trfrag(b, b + 1024);
                }
        if (OWNLN) {
#pragma unroll
            for (int i = 0; i < 4; ++i) {
                const int ks = 2 * c + (i >> 1), tt = i & 1;
                const bf8 xv = asbf(lds16(XB0 + (tt * 16 + ks) * 1024 + lane * 16));
                acc[0][tt] = mfma(W[0][ks], xv, acc[0][tt]); acc[1][tt] = mfma(W[1][ks], xv, acc[1][tt]);
                acc[2][tt] = mfma(xv, W[2][ks], acc[2][tt]); acc[3][tt] = mfma(xv, W[3][ks], acc[3][tt]);
            }
        }
    }
    lds_barrier();
}

template <int MODE>
DI void phase_proj(const Par& p, u64 tg, int grp, int first, int second, int stride, int wave, int lane) {
    constexpr bool KV = MODE == 0, FF = MODE == 1;
    char* ws = (char*)p.ws;
    bf8 W[4][16];
    f4 acc[4][2];
#pragma unroll
    for (int i = 0; i < 4; ++i) { acc[i][0] = (f4){0, 0, 0, 0}; acc[i][1] = (f4){0, 0, 0, 0}; }
    if (KV) load_w2<true, false, true>(p, tg, first, second, W, acc, (const char*)p.wk + grp * 256, (const char*)p.wv + grp * 256, 8192, 16, wave, lane);
    else if (FF) load_w2<true, true, false>(p, tg, first, first, W, acc, (const char*)p.wg + grp * 256, (const char*)p.wu + grp * 256, 2752, grp == 10 ? 12 : 16, wave, lane);
    else load_w2<true, true, false>(p, tg, first, first, W, acc, (const char*)p.wq + grp * 512, (const char*)p.wq + grp * 512 + 256, 8192, 16, wave, lane);
    const __amdgpu_buffer_rsrc_t ro = KV ? rsrc(ws + WS_KV + (size_t)grp * MB1, MB1) : FF ? rsrc(ws + WS_HF, 64 * 2 * 43 * 1024) : rsrc(ws + WS_QF + (size_t)grp * MB1, MB1);
    auto flag = [&](int s) {
        if (s >= 0 && wave == 0) {
            setflag(ws + WS_FLAGS + (FF ? F_H + s * 128 + grp * 8 : KV ? F_KV + grp * 1024 + s * 16 : F_Q + grp * 512 + s * 8), tg);
        }
    };
    bool ln_all = false;
    auto ln_wait = [&](int s) {
        if (!ln_all) { ln_all = __all(ldflag(ws + WS_FLAGS + F_LN + lane * 8) == tg); if (!ln_all) wait_set(ws + WS_FLAGS + F_LN + s * 8, true, tg); }
    };
    constexpr int NST = FF ? 2 : 4;
    const bool live = !FF || grp * 4 + wave < 43;
    u4 pk[NST];
    auto store = [&](int i, int s) {
        const unsigned vo = (s >= 0 && live) ? lane * 16 : 0x80000000u;
        s = s < 0 ? 0 : s;
        if (KV) st16(ro, pk[i], vo, s * 16384 + (i < 2 ? 4096 + (i * 4 + wave) * 1024 : (2 * wave + i - 2) * 1024 + (wave < 2 ? 0 : 8192)));
        else if (FF) st16(ro, pk[i], vo, ((s * 2 + i) * 43 + grp * 4 + wave) * 1024);
        else st16(ro, pk[i], vo, (i >> 1) * 524288 + s * 8192 + ((i & 1) * 4 + wave) * 1024);
    };
    auto pack1 = [&](int d) {
        const int i = d >> 2, j = (d & 1) * 2, hi = (d >> 1) & 1;
        if (FF) pk[i][d & 3] = pk2(rbf(acc[hi][i][j]) * rbf(acc[2 + hi][i][j]), rbf(acc[hi][i][j + 1]) * rbf(acc[2 + hi][i][j + 1]));
        else { const f4 a = (KV && i >= 2) ? acc[i][hi] : acc[(KV ? 0 : 2 * (i >> 1)) + hi][i & 1]; pk[i][d & 3] = pk2(a[j], a[j + 1]); }
    };
    auto pack = [&]() {
#pragma unroll
        for (int d = 0; d < 4 * NST; ++d) pack1(d);
    };
    pack();
    if (KV) {
#pragma unroll
        for (int i = 0; i < NST; ++i) store(i, first);
        drain(); lds_barrier(); flag(first);
        first = second; second += stride;
    }
    int buf = 0, pend = -1, p1 = -1, p2 = -1;
    bool fresh = true;
    for (int seg = first, nxt = second; seg < 64; seg = nxt, nxt += stride) {
        if (fresh) drain(); else if (FF) asm volatile("s_waitcnt vmcnt(2)" ::: "memory"); else asm volatile("s_waitcnt vmcnt(4)" ::: "memory");
        lds_barrier();
        flag(p2);
        const bool lead = KV && seg < 7;
        const int ns = nxt < 64 ? nxt : seg;
        ln_wait(ns);
        const char* xf = ws + WS_XF + (size_t)ns * 32768 + wave * 8192;
        const unsigned lb = (buf ? 0u : XB0) + wave * 8192;
#pragma unroll
        for (int i = 0; i < 4; ++i) { acc[i][0] = (f4){0, 0, 0, 0}; acc[i][1] = (f4){0, 0, 0, 0}; }
        const unsigned xb = (buf ? XB0 : 0u) + lane * 16;
        u4 xr[4];
#pragma unroll
        for (int i = 0; i < 3; ++i) xr[i] = lds16(xb + ((i & 1) * 16 + (i >> 1)) * 1024);
        __builtin_amdgcn_sched_barrier(0);
        sfor<0, 32>([&](auto I) {
            constexpr int i = I, ks = i >> 1, tt = i & 1;
            if constexpr (i + 3 < 32) xr[(i + 3) & 3] = lds16(xb + (((i + 3) & 1) * 16 + ((i + 3) >> 1)) * 1024);
            const bf8 xv = asbf(xr[i & 3]);
            acc[0][tt] = mfma(W[0][ks], xv, acc[0][tt]);
            acc[1][tt] = mfma(W[1][ks], xv, acc[1][tt]);
            if constexpr (i < 16 && !(i & 1)) glds4<(i >> 1)>(i < 8 ? xf : xf + 4096, lane * 16, lb);
            if constexpr (i >= 17 && (i & 3) == 1 && (i - 17) / 4 < NST) store((i - 17) / 4, pend);
            if (KV) { acc[2][tt] = mfma(xv, W[2][ks], acc[2][tt]); acc[3][tt] = mfma(xv, W[3][ks], acc[3][tt]); }
            else { acc[2][tt] = mfma(W[2][ks], xv, acc[2][tt]); acc[3][tt] = mfma(W[3][ks], xv, acc[3][tt]); }
            __builtin_amdgcn_sched_barrier(0);
        });
        pack();
        p2 = p1; p1 = pend; pend = seg; fresh = false;
        if (lead) {
#pragma unroll
            for (int i = 0; i < NST; ++i) store(i, seg);
            drain(); lds_barrier(); flag(seg); pend = -1; fresh = true;
        }
        buf ^= 1;
    }
#pragma unroll
    for (int i = 0; i < NST; ++i) store(i, pend);
    drain();
    lds_barrier();
    flag(p2); flag(p1); flag(pend);
}

DI f2 swap16(float v) { float w = v; asm volatile("s_nop 1\n\tv_permlane16_swap_b32 %0, %1" : "+v"(v), "+v"(w)); return (f2){v, w}; }
DI f2 swap32(float v) { float w = v; asm volatile("s_nop 1\n\tv_permlane32_swap_b32 %0, %1" : "+v"(v), "+v"(w)); return (f2){v, w}; }
DI float xg_max(float v) { f2 a = swap16(v); f2 b = swap32(fmaxf(a[0], a[1])); return fmaxf(b[0], b[1]); }
DI float xg_sum(float v) { f2 a = swap16(v); f2 b = swap32(a[0] + a[1]); return b[0] + b[1]; }

DI void phase_attn(const Par& p, u64 tg, int h, int first, int last, int stride, int wave, int lane) {
    char* ws = (char*)p.ws;
    const int lo = lane & 15, g = lane >> 4, q = lo >> 2, pp = lo & 3;
    const unsigned scr = wave * 6144;
    const __amdgpu_buffer_rsrc_t rq = rsrc(ws + WS_QF + (size_t)h * 524288, 524288), rkv = rsrc(ws + WS_KV + (size_t)h * MB1, MB1),
                                 rm = rsrc(ws + WS_KVM + (size_t)h * 524288, 524288), rof = rsrc(ws + WS_OF, 16 * MB1);
    const float c = 0.12751743f;
    int ready = 0;
    u4 NQ[8], NK[8], NV[8];
    auto fetch = [&](int seg) {
        for (unsigned s = 0; s < SPIN_CAP && seg >= ready; ++s) {
            const u64 a = ldflag(ws + WS_FLAGS + F_Q + (h >> 1) * 512 + lane * 8), b = ldflag(ws + WS_FLAGS + F_KV + h * 1024 + lane * 16);
            const u64 miss = __ballot(a != tg || b != tg);
            ready = miss ? __builtin_ctzll(miss) : 64;
            if (seg >= ready) __builtin_amdgcn_s_sleep(16);
        }
        asm volatile("" ::: "memory");
#pragma unroll
        for (int i = 0; i < 8; ++i) NQ[i] = ld16(rq, lane * 16, seg * 8192 + i * 1024);
#pragma unroll
        for (int i = 0; i < 8; ++i) NK[i] = ld16(rkv, lane * 16, seg * 16384 + 4096 + i * 1024);
#pragma unroll
        for (int i = 0; i < 8; ++i) NV[i] = ld16(rkv, lane * 16, seg * 16384 + (i < 4 ? i * 1024 : 8192 + i * 1024));
        __builtin_amdgcn_sched_barrier(0);
    };
    fetch(first);
    for (int seg = first; seg <= last; seg += stride) {
        bf8 QF[4][2], KF[2][4], VF[8];
#pragma unroll
        for (int i = 0; i < 8; ++i) { QF[i & 3][i >> 2] = asbf(NQ[i]); KF[i >> 2][i & 3] = asbf(NK[i]); VF[i] = asbf(NV[i]); }
        f4 S00 = {0, 0, 0, 0}, S01 = S00, S11 = S00;
#pragma unroll
        for (int u = 0; u < 4; ++u) { S00 = mfma(KF[0][u], QF[u][0], S00); S01 = mfma(KF[0][u], QF[u][1], S01); S11 = mfma(KF[1][u], QF[u][1], S11); }
        wait_prog(ws + WS_FLAGS + F_PROG + (2 * h + (lane & 1)) * 256, seg + 1, tg);
        u4 r0[4], r1[4];
#pragma unroll
        for (int w = 0; w < 4; ++w) { r0[w] = ld16(rm, lane * 16, seg * 4096 + w * 1024); r1[w] = ld16(rm, lane * 16, 262144 + seg * 4096 + w * 1024); }
        __builtin_amdgcn_sched_barrier(0);
        const bool more = seg + stride <= last, pre = more && seg + stride < ready;
        if (pre) fetch(seg + stride);
#pragma unroll
        for (int w = 0; w < 4; ++w) {
            const unsigned a = scr + w * 1536 + lo * 32 + (lo >> 3) * 128 + g * 8;
            *(LAS unsigned*)a = r0[w][0]; *(LAS unsigned*)(a + 768) = r0[w][1];
        }
        asm volatile("s_waitcnt lgkmcnt(0)" ::: "memory");
        f4 M0 = {0, 0, 0, 0}, M1 = M0;
#pragma unroll
        for (int u = 0; u < 4; ++u) {
            const unsigned a = scr + u * 1536 + g * 384 + q * 32 + pp * 8;
            const bf8 km = trfrag(a, a + 128);
            M0 = mfma(km, QF[u][0], M0); M1 = mfma(km, QF[u][1], M1);
        }
        asm volatile("s_waitcnt lgkmcnt(0)" ::: "memory");
        bf8 PX[2], PM[2];
#pragma unroll
        for (int tt = 0; tt < 2; ++tt) {
            float s[10];
            const f4 D = tt ? S11 : S00, Mm = tt ? M1 : M0;
#pragma unroll
            for (int j = 0; j < 4; ++j) {
                s[j] = tt ? S01[j] : -3e38f;
                s[4 + j] = (4 * g + j <= lo) ? D[j] : -3e38f;
            }
            s[8] = Mm[0]; s[9] = Mm[1];
            float mx = fmaxf(s[8], s[9]);
#pragma unroll
            for (int j = tt ? 0 : 4; j < 8; ++j) mx = fmaxf(mx, s[j]);
            mx = xg_max(mx);
            float l = 0.f;
#pragma unroll
            for (int j = 0; j < 10; ++j) { s[j] = (j < 4 && !tt) ? 0.f : __builtin_amdgcn_exp2f((s[j] - mx) * c); l += s[j]; }
            const float inv = __builtin_amdgcn_rcpf(xg_sum(l));
            if (tt) PX[1] = asbf((u4){pk2(s[0] * inv, s[1] * inv), pk2(s[2] * inv, s[3] * inv), pk2(s[4] * inv, s[5] * inv), pk2(s[6] * inv, s[7] * inv)});
            else PX[0] = asbf((u4){pk2(s[4] * inv, s[5] * inv), pk2(s[6] * inv, s[7] * inv), 0u, 0u});
            PM[tt] = asbf((u4){pk2(s[8] * inv, s[9] * inv), 0u, 0u, 0u});
        }
#pragma unroll
        for (int x = 0; x < 4; ++x) {
            f4 O[2][2];
#pragma unroll
            for (int a = 0; a < 2; ++a) {
                const int et = 2 * x + a;
                const bf8 vm = asbf((u4){et < 4 ? r0[et & 3][2] : r1[et & 3][2], 0u, 0u, 0u});
#pragma unroll
                for (int tt = 0; tt < 2; ++tt) O[a][tt] = mfma(vm, PM[tt], mfma(VF[et], PX[tt], (f4){0, 0, 0, 0}));
            }
#pragma unroll
            for (int tt = 0; tt < 2; ++tt)
                st16(rof, (u4){pk2(O[0][tt][0], O[0][tt][1]), pk2(O[0][tt][2], O[0][tt][3]), pk2(O[1][tt][0], O[1][tt][1]), pk2(O[1][tt][2], O[1][tt][3])},
                     lane * 16, ((seg * 2 + tt) * 128 + 4 * h + x) * 1024);
        }
        drain();
        setflag(ws + WS_OFL + ((seg * 4 + (h >> 3)) * 16 + (h & 7)) * 128, tg);
        if (more && !pre) fetch(seg + stride);
    }
}

DI void attn_tail(const Par& p, u64 tg, int h, int seg, int tt, int wave, int lane) {
    char* ws = (char*)p.ws;
    const int lo = lane & 15, g = lane >> 4, q = lo >> 2, pp = lo & 3;
    const unsigned scr = wave * 6144;
    const __amdgpu_buffer_rsrc_t rq = rsrc(ws + WS_QF + (size_t)h * 524288, 524288), rkv = rsrc(ws + WS_KV + (size_t)h * MB1, MB1),
                                 rm = rsrc(ws + WS_KVM + (size_t)h * 524288, 524288), rof = rsrc(ws + WS_OF, 16 * MB1);
    const float c = 0.12751743f;
    wait_set<16>(ws + WS_FLAGS + ((lane & 1) ? F_Q + (h >> 1) * 512 + seg * 8 : F_KV + h * 1024 + seg * 16), true, tg);
    bf8 QF[4], KF[2][4], VF[8];
#pragma unroll
    for (int i = 0; i < 4; ++i) QF[i] = asbf(ld16(rq, lane * 16, seg * 8192 + (tt * 4 + i) * 1024));
#pragma unroll
    for (int i = 0; i < 8; ++i) KF[i >> 2][i & 3] = asbf(ld16(rkv, lane * 16, seg * 16384 + 4096 + i * 1024));
#pragma unroll
    for (int i = 0; i < 8; ++i) VF[i] = asbf(ld16(rkv, lane * 16, seg * 16384 + (i < 4 ? i * 1024 : 8192 + i * 1024)));
    __builtin_amdgcn_sched_barrier(0);
    f4 S0 = {0, 0, 0, 0}, S1 = S0;
#pragma unroll
    for (int u = 0; u < 4; ++u) { S0 = mfma(KF[0][u], QF[u], S0); S1 = mfma(KF[1][u], QF[u], S1); }
    float e[8], mx = -3e38f, l = 0.f;
#pragma unroll
    for (int j = 0; j < 8; ++j) {
        const int key = 16 * (j >> 2) + 4 * g + (j & 3);
        e[j] = key <= 16 * tt + lo ? (j < 4 ? S0[j & 3] : S1[j & 3]) : -3e38f;
        mx = fmaxf(mx, e[j]);
    }
    mx = xg_max(mx);
#pragma unroll
    for (int j = 0; j < 8; ++j) { e[j] = __builtin_amdgcn_exp2f((e[j] - mx) * c); l += e[j]; }
    const bf8 PX = asbf((u4){pk2(e[0], e[1]), pk2(e[2], e[3]), pk2(e[4], e[5]), pk2(e[6], e[7])});
    f4 O[8];
#pragma unroll
    for (int et = 0; et < 8; ++et) O[et] = mfma(VF[et], PX, (f4){0, 0, 0, 0});
    const char* pg = ws + WS_FLAGS + F_PROG + (2 * h + (lane & 1)) * 256;
    wait_prog(pg, seg == 61 ? 60 : 61, tg);
    if (seg != 61) __builtin_amdgcn_s_sleep(TAIL_NAP);
    u4 r0[4], r1[4];
    for (unsigned s = 0; s < SPIN_CAP; ++s) {
        const bool late = seg == 61 && __all(count_of(ldflag(pg), tg) >= 62);
        r1[3] = ld16(rm, lane * 16, 262144 + seg * 4096 + 3072);
        if (late || __all(r1[3][2] != ~0u)) {
#pragma unroll
            for (int w = 0; w < 4; ++w) { r0[w] = ld16(rm, lane * 16, seg * 4096 + w * 1024); if (w < 3) r1[w] = ld16(rm, lane * 16, 262144 + seg * 4096 + w * 1024); }
            if (late) r1[3] = ld16(rm, lane * 16, 262144 + seg * 4096 + 3072);
            __builtin_amdgcn_sched_barrier(0);
            bool ok = true;
#pragma unroll
            for (int w = 0; w < 4; ++w) ok = ok && r0[w][0] != ~0u && r0[w][1] != ~0u && r0[w][2] != ~0u && r1[w][2] != ~0u;
            if (late || __all(ok)) break;
        }
        asm volatile("s_sleep 2" ::: "memory");
    }
#pragma unroll
    for (int w = 0; w < 4; ++w) {
        const unsigned a = scr + w * 1536 + lo * 32 + (lo >> 3) * 128 + g * 8;
        *(LAS unsigned*)a = r0[w][0]; *(LAS unsigned*)(a + 768) = r0[w][1];
    }
    asm volatile("s_waitcnt lgkmcnt(0)" ::: "memory");
    f4 M = {0, 0, 0, 0};
#pragma unroll
    for (int u = 0; u < 4; ++u) { const unsigned a = scr + u * 1536 + g * 384 + q * 32 + pp * 8; M = mfma(trfrag(a, a + 128), QF[u], M); }
    const float m0 = M[0], m1 = M[1], mn = fmaxf(mx, xg_max(fmaxf(m0, m1)));
    const float f = __builtin_amdgcn_exp2f((mx - mn) * c), e0 = __builtin_amdgcn_exp2f((m0 - mn) * c), e1 = __builtin_amdgcn_exp2f((m1 - mn) * c);
    float inv = __builtin_amdgcn_rcpf(xg_sum(l * f + e0 + e1));
    const float sc = f * inv;
    const bf8 PM = asbf((u4){pk2(e0 * inv, e1 * inv), 0u, 0u, 0u});
#pragma unroll
    for (int x = 0; x < 4; ++x) {
        const f4 a = mfma(asbf((u4){x < 2 ? r0[(2 * x) & 3][2] : r1[(2 * x) & 3][2], 0u, 0u, 0u}), PM, O[2 * x] * sc);
        const f4 b = mfma(asbf((u4){x < 2 ? r0[(2 * x + 1) & 3][2] : r1[(2 * x + 1) & 3][2], 0u, 0u, 0u}), PM, O[2 * x + 1] * sc);
        st16(rof, (u4){pk2(a[0], a[1]), pk2(a[2], a[3]), pk2(b[0], b[1]), pk2(b[2], b[3])}, lane * 16, ((seg * 2 + tt) * 128 + 4 * h + x) * 1024);
    }
    drain();
    setflag(ws + WS_OFL + ((seg * 4 + (h >> 3)) * 16 + tt * 8 + (h & 7)) * 128, tg);
}

DI void phase_out(const Par& p, u64 tg, int sg, bool tail, int kq, int cg, int wave, int lane) {
    auto next = [&](int s) { return tail ? (s < 54 ? s + 2 : s + 8) : s >= 57 ? 64 : (s >= 47 && s < 50) ? s + 9 : s + 3; };
    char* ws = (char*)p.ws;
    const int lo = lane & 15, g = lane >> 4, q = lo >> 2, pp = lo & 3;
    bf8 WO[2][32], WD[2][11];
    const unsigned wb = wave * 40960;
    {
        const char* m = (const char*)p.wo + (size_t)kq * 1024 * 1024 + cg * 256 + wave * 64;
        const unsigned src = (lane >> 2) * 1024 + ((lane & 3) ^ (2 * ((lane >> 4) & 1))) * 16;
        const unsigned ad = wb + (4 * g + q) * 64 + (((pp >> 1)) ^ (2 * (g & 1))) * 16 + (pp & 1) * 8;
#pragma unroll
        for (int part = 0; part < 4; ++part) {
            for (int n = 0; n < 16; ++n) glds<false>(m + (size_t)(part * 256 + n * 16) * 1024, src, wb + n * 1024);
            drain();
#pragma unroll
            for (int k = 0; k < 8; ++k)
#pragma unroll
                for (int ct = 0; ct < 2; ++ct) WO[ct][part * 8 + k] = trfrag((ad ^ (ct * 32)) + k * 2048, (ad ^ (ct * 32)) + k * 2048 + 1024);
            asm volatile("s_waitcnt lgkmcnt(0)" ::: "memory");
        }
        const char* md = (const char*)p.wd + cg * 256 + wave * 64;
        const unsigned srd = (lane >> 2) * 1024 + ((lane & 3) ^ (2 * ((lane >> 5) & 1))) * 16;
        const unsigned add = wb + (8 * g + q) * 64 + (((pp >> 1)) ^ (2 * (g & 1))) * 16 + (pp & 1) * 8;
        for (int n = 0; n < 22; ++n) { int row = kq * 352 + n * 16; if (row > 1360) row = 1360; glds<false>(md + (size_t)row * 1024, srd, wb + n * 1024); }
        drain();
#pragma unroll
        for (int k = 0; k < 11; ++k)
#pragma unroll
            for (int ct = 0; ct < 2; ++ct) WD[ct][k] = trfrag((add ^ (ct * 32)) + k * 2048, (add ^ (ct * 32)) + k * 2048 + 256);
        lds_barrier();
    }
    const int nh = kq < 3 ? 11 : 10;
    const __amdgpu_buffer_rsrc_t rpx = rsrc(ws + WS_PX, 32 * MB1);
    const int ct_e = kq >> 1, tt_e = kq & 1;
    const unsigned ovo = lane * 16 + ((wave & 1) * 128 + (wave >> 1)) * 1024;
    auto fetch_h = [&](int seg) {
        wait_set(ws + WS_FLAGS + F_H + seg * 128 + (lane & 15) * 8, (lane & 15) < 11, tg);
        for (int i = wave; i < 2 * nh; i += 4) glds<true>(ws + WS_HF + ((size_t)(seg * 2 + (i & 1)) * 43 + kq * 11 + (i >> 1)) * 1024, lane * 16, 65536 + i * 1024);
    };
    unsigned sv[4];
#pragma unroll
    for (int i = 0; i < 4; ++i) sv[i] = i == kq ? 0x80000000u : lane * 16 + i * 8192 + ((kq - i - 1) & 3) * 2048;
    f4 so, dn;
    auto parts = [&](int seg, int off, f4 own) {
        const unsigned pb = ((seg * 4 + cg) * 4 + wave) * 32768 + kq * 8192 + off;
        f4 v[3];
#pragma unroll
        for (int k = 0; k < 3; ++k) v[k] = __builtin_bit_cast(f4, ld16(rpx, lane * 16 + k * 2048, pb));
        return own + (v[0] + (v[1] + v[2]));
    };
    auto eoff = [&](int seg) { return (size_t)(seg * 32 + tt_e * 16 + lo) * 1024 + cg * 256 + wave * 64 + ct_e * 32 + g * 8; };
    auto gate = [&](f4 d) {
        f4 r;
#pragma unroll
        for (int j = 0; j < 4; ++j) { const float db = rbf(d[j]); r[j] = rbf(db / (1.f + __expf(-db))); }
        return r;
    };
    auto emit = [&](int seg, u2 xv, f4 s, f4 gt) {
        float o[4];
#pragma unroll
        for (int j = 0; j < 4; ++j) o[j] = ((j & 1) ? bfhi(xv[j >> 1]) : bflo(xv[j >> 1])) + rbf(rbf(s[j]) * gt[j]);
        __builtin_nontemporal_store((u2){pk2(o[0], o[1]), pk2(o[2], o[3])}, (u2*)((char*)p.out + eoff(seg)));
    };
    auto xflag = [&](int seg) { return ws + WS_FLAGS + F_X + ((seg * 4 + cg) * 4 + wave) * 64; };
    auto finish = [&](int seg) {
        const u2 xv = *(const u2*)((const char*)p.x + eoff(seg));
        wait_set<1>(xflag(seg) + (lane & 3) * 8, (lane & 3) != kq, tg);
        const f4 s = parts(seg, 0, so), d = parts(seg, 1024, dn);
        __builtin_amdgcn_sched_barrier(0);
        emit(seg, xv, s, gate(d));
    };
    auto send = [&](int seg, const f4 (&T)[2][2], int off, int mode) {
        const unsigned pb = ((seg * 4 + cg) * 4 + wave) * 32768 + off;
#pragma unroll
        for (int i = 0; i < 4; ++i) {
            u4 v = __builtin_bit_cast(u4, T[i >> 1][i & 1]);
#pragma unroll
            for (int j = 0; j < 4; ++j) v[j] = mode == 1 ? ~0u : mode == 2 ? (v[j] < ~1u ? v[j] : ~1u) : v[j];
            st16(rpx, v, sv[i], pb);
        }
    };
    auto own = [&](const f4 (&T)[2][2]) { return kq == 0 ? T[0][0] : kq == 1 ? T[0][1] : kq == 2 ? T[1][0] : T[1][1]; };
    fetch_h(sg);
    drain();
    int done = -1;
    for (int seg = sg; seg < 64; seg = next(seg)) {
        f4 D[2][2] = {{{0, 0, 0, 0}, {0, 0, 0, 0}}, {{0, 0, 0, 0}, {0, 0, 0, 0}}}, S[2][2] = {{{0, 0, 0, 0}, {0, 0, 0, 0}}, {{0, 0, 0, 0}, {0, 0, 0, 0}}};
        lds_barrier();
        const char* of_p = ws + WS_OFL + ((seg * 4 + kq) * 16 + (lane & (seg > 60 ? 15 : 7))) * 128;
        const u64 oflag = ldflag(of_p);
        {
            u4 hf[4];
#pragma unroll
            for (int i = 0; i < 3; ++i) hf[i] = lds16(65536 + i * 1024 + lane * 16);
            __builtin_amdgcn_sched_barrier(0);
#pragma unroll
            for (int i = 0; i < 22; ++i) {
                if (i + 3 < 22) hf[(i + 3) & 3] = lds16(65536 + (i + 3) * 1024 + lane * 16);
                if (i < 20 || nh == 11) { D[0][i & 1] = mfma(WD[0][i >> 1], asbf(hf[i & 3]), D[0][i & 1]); D[1][i & 1] = mfma(WD[1][i >> 1], asbf(hf[i & 3]), D[1][i & 1]); }
                __builtin_amdgcn_sched_barrier(0);
            }
        }
        const bool last = next(seg) >= 64;
        const bool early = !last && __all(oflag == tg);
        u2 xl = {0, 0}; f4 gl = {0, 0, 0, 0};
        if (!early) {
            if (done >= 0) finish(done);
            done = -1;
            if (last) {
                send(seg, D, 1024, 0); send(seg, S, 0, 1);
                xl = *(const u2*)((const char*)p.x + eoff(seg));
                drain();
                setflag(xflag(seg) + kq * 8, tg);
                wait_set<1>(xflag(seg) + (lane & 3) * 8, (lane & 3) != kq, tg);
                gl = gate(parts(seg, 1024, own(D)));
            }
            if (wave == 0) {
                doze_until<64>((char*)p.prog + kq * 4096, (seg < 61 ? seg : 61) - 1, tg);
                wait_set<2>(of_p, true, tg);
            }
        }
        if (!early || true) lds_barrier();
        const char* ob = ws + WS_OF + ((size_t)seg * 256 + kq * 32) * 1024;
        sfor<0, 16>([&](auto I) { gldsn<I * 4096>(ob + I * 2048, ovo, wave * 1024); });
#pragma unroll
        for (int st = 0; st < 4; ++st) {
            if (st == 0) asm volatile("s_waitcnt vmcnt(12)" ::: "memory");
            else if (st == 1) asm volatile("s_waitcnt vmcnt(8)" ::: "memory");
            else if (st == 2) asm volatile("s_waitcnt vmcnt(4)" ::: "memory");
            else drain();
            lds_barrier();
            if (st == 3 && next(seg) < 64) fetch_h(next(seg));
            u4 of[4];
#pragma unroll
            for (int j = 0; j < 3; ++j) of[j] = lds16((st * 16 + j) * 1024 + lane * 16);
            __builtin_amdgcn_sched_barrier(0);
#pragma unroll
            for (int j = 0; j < 16; ++j) {
                if (j + 3 < 16) of[(j + 3) & 3] = lds16((st * 16 + j + 3) * 1024 + lane * 16);
                S[0][j & 1] = mfma(WO[0][st * 8 + (j >> 1)], asbf(of[j & 3]), S[0][j & 1]); S[1][j & 1] = mfma(WO[1][st * 8 + (j >> 1)], asbf(of[j & 3]), S[1][j & 1]);
                __builtin_amdgcn_sched_barrier(0);
            }
        }
        if (last) {
            send(seg, S, 0, 2);
            __builtin_amdgcn_s_sleep(PX_NAP);
            const unsigned pb = ((seg * 4 + cg) * 4 + wave) * 32768 + kq * 8192;
            u4 v[3];
            for (unsigned sp = 0; sp < SPIN_CAP; ++sp) {
#pragma unroll
                for (int k = 0; k < 3; ++k) v[k] = ld16(rpx, lane * 16 + k * 2048, pb);
                unsigned m = 0;
#pragma unroll
                for (int k = 0; k < 3; ++k) m = max(m, max(max(v[k][0], v[k][1]), max(v[k][2], v[k][3])));
                if (__all(m != ~0u)) break;
                asm volatile("" ::: "memory");
            }
            emit(seg, xl, own(S) + (__builtin_bit_cast(f4, v[0]) + (__builtin_bit_cast(f4, v[1]) + __builtin_bit_cast(f4, v[2]))), gl);
            return;
        }
        send(seg, S, 0, 0); send(seg, D, 1024, 0);
        if (done >= 0) finish(done);
        so = own(S); dn = own(D);
        drain();
        setflag(xflag(seg) + kq * 8, tg);
        done = seg;
    }
}

DI void poison(const Par& p, u64 tg, int j, int wave, int lane) {
    char* ws = (char*)p.ws;
    const __amdgpu_buffer_rsrc_t r = rsrc(ws, 4 * MB1);
    const u4 ones = {~0u, ~0u, ~0u, ~0u};
#pragma unroll
    for (int i = 0; i < 13; ++i) st16(r, ones, lane * 16, j * 53248 + (wave * 13 + i) * 1024);
    st16(r, ones, lane * 16, WS_TS + j * 4096 + wave * 1024);
    const __amdgpu_buffer_rsrc_t rk = rsrc(ws + WS_KVM + (size_t)j * 262144, 262144);
    st16(rk, ones, lane * 16, 61 * 4096 + wave * 1024); st16(rk, ones, lane * 16, 62 * 4096 + wave * 1024); st16(rk, ones, lane * 16, 63 * 4096 + wave * 1024);
    drain();
    lds_barrier();
    if (wave == 0) setflag(ws + WS_FLAGS + F_POI + j * 8, tg);
}

DI void probe_page(const Par& p, u64 tg, int pg, int lane) {
    char* ws = (char*)p.ws;
    const char* b = ws + pg * 2 * MB1;
    st16(rsrc(b, 49152), (u4){~0u, ~0u, ~0u, ~0u}, (lane >> 3) * 8192 + 8064 + (lane & 7) * 16, 0);
    drain();
    unsigned best[2] = {~0u, ~0u};
    for (int rep = 0; rep < 3; ++rep)
#pragma unroll
        for (int par = 0; par < 2; ++par) {
            const unsigned off = par * 8192 + rep * 16384 + 8064 + (lane & 7) * 16;
            unsigned a; unsigned long long c0, c1;
            asm volatile("s_memtime %1\n\ts_waitcnt lgkmcnt(0)\n\tglobal_load_dword %0, %3, %4 sc1\n\ts_waitcnt vmcnt(0)\n\ts_memtime %2\n\ts_waitcnt lgkmcnt(0)"
                         : "=&v"(a), "=&s"(c0), "=&s"(c1) : "v"(off), "s"(b) : "memory");
            const unsigned d = (unsigned)(c1 - c0);
            best[par] = d < best[par] ? d : best[par];
        }
    setflag(ws + WS_FLAGS + F_PAR + pg * 8, tg + (best[0] > best[1] ? 1u : 0u));
}

DI bool census(const char* fl, int lane, u64 tg, unsigned me, unsigned& slot) {
    u64 lost[4], open[4];
    int k = 0, nlost = 0, nopen = 0;
#pragma unroll
    for (int i = 0; i < 4; ++i) lost[i] = __ballot(ldflag(fl + F_LOSE + (i * 64 + lane) * 8) == tg);
#pragma unroll
    for (int i = 0; i < 4; ++i) open[i] = __ballot(ldflag(fl + F_SLOT + (i * 64 + lane) * 64) != tg);
#pragma unroll
    for (int i = 0; i < 4; ++i) {
        nlost += __builtin_popcountll(lost[i]); nopen += __builtin_popcountll(open[i]);
        const int below = (int)me - i * 64;
        k += __builtin_popcountll(below >= 64 ? lost[i] : below <= 0 ? 0ull : lost[i] & ((1ull << below) - 1));
    }
#pragma unroll
    for (int i = 0; i < 4; ++i) {
        const int n = __builtin_popcountll(open[i]);
        if (k >= 0 && k < n) { u64 fr = open[i]; for (int j = 0; j < k; ++j) fr &= fr - 1; slot = i * 64 + __builtin_ctzll(fr); }
        k -= n;
    }
    return nopen == nlost;
}
DI int claim_slot(const Par& p, int wave, int lane, u64& tg) {
    char* fl = (char*)p.ws + WS_FLAGS;
    if (wave == 0) {
        const u64 e = ldflag(fl + F_EPOCH);
        const u64 t = (((u64)rfl((unsigned)(e >> 32)) << 32 | rfl((unsigned)e)) & ~127ull) + 128;
        unsigned slot = (p.pad & 1) ? blockIdx.x : (blockIdx.x & ~7u) | (__builtin_amdgcn_s_getreg(20 | (3 << 11)) & SLOT_XMASK);
        if (!(p.pad & 1)) {
            u64 keep, old;
            asm volatile("s_mov_b64 %0, exec\n\ts_mov_b64 exec, 1\n\tglobal_atomic_swap_x2 %1, %2, %3, %4 sc0 sc1\n\ts_waitcnt vmcnt(0)\n\ts_mov_b64 exec, %0"
                         : "=&s"(keep), "=&v"(old) : "v"(0u), "v"(t), "s"(fl + F_SLOT + slot * 64) : "memory");
            if (rfl((unsigned)old) == (unsigned)t && rfl((unsigned)(old >> 32)) == (unsigned)(t >> 32)) {
                setflag(fl + F_LOSE + blockIdx.x * 8, t);
                for (unsigned s = 0; s < SPIN_CAP && !census(fl, lane, t, blockIdx.x, slot); ++s) __builtin_amdgcn_s_sleep(8);
            }
        }
        *(LAS u4*)64u = (u4){slot, 0u, (unsigned)t, (unsigned)(t >> 32)};
    }
    lds_barrier();
    const u4 r = *(LAS u4*)64u;
    const unsigned s = rfl(r[0]);
    tg = (u64)rfl(r[3]) << 32 | rfl(r[2]);
    lds_barrier();
    return (int)s;
}

__global__ void __launch_bounds__(256, 1) rmt_kernel(Par p) {
    const int wave = rfl(threadIdx.x >> 6), lane = threadIdx.x & 63;
    u64 tg;
    const int b = claim_slot(p, wave, lane, tg), x = b & 7, r = b >> 3;
    if (x < 4 && r < 16) {
        if (!(p.phases & 16)) return;
        const int cu = ((r >> 1) * 4 + x) * 2 + (r & 1);
        const int ids = cu | wave << 8 | (__builtin_amdgcn_s_getreg(20 | (3 << 11)) & 3) << 16;
        const void* par = (const void*)__builtin_amdgcn_kernarg_segment_ptr();
        asm volatile(
"s_load_dwordx8 s[56:63], %[par], 0\n"
"s_load_dwordx2 s[64:65], %[par], 32\n"
"s_load_dwordx2 s[46:47], %[par], 40\n"
"s_load_dwordx2 s[50:51], %[par], 56\n"
"s_load_dwordx2 s[52:53], %[par], 64\n"
"s_load_dwordx2 s[82:83], %[par], 96\n"
"s_load_dwordx2 s[84:85], %[par], 104\n"
"s_load_dwordx4 s[36:39], %[par], 72\n"
"s_load_dwordx2 s[8:9], %[par], 48\n"
"s_and_b32 s40, %[ids], 0xff\n"
"s_bfe_u32 s41, %[ids], 0x80008\n"
"s_lshr_b32 s43, %[ids], 16\n"
"s_waitcnt lgkmcnt(0)\n"
"s_lshr_b32 s44, s40, 1\n"
"s_lshl_b32 s44, s44, 10\n"
"s_add_u32 s8, s8, s44\n"
"s_addc_u32 s9, s9, 0\n"
"s_memtime s[12:13]\n"
"s_nop 3\n"
"s_memrealtime s[14:15]\n"
"s_nop 3\n"
"s_memrealtime s[16:17]\n"
"s_nop 3\n"
"s_memrealtime s[18:19]\n"
"s_nop 3\n"
"s_memrealtime s[20:21]\n"
"s_nop 3\n"
"s_memrealtime s[22:23]\n"
"s_nop 3\n"
"s_memrealtime s[24:25]\n"
"s_nop 3\n"
"s_waitcnt lgkmcnt(0)\n"
"s_mov_b32 s87, s12\n"
"s_mov_b32 s13, 0\n"
"s_cmp_eq_u32 s14, s16\n"
"s_addc_u32 s13, s13, 0\n"
"s_cmp_eq_u32 s14, s18\n"
"s_addc_u32 s13, s13, 0\n"
"s_cmp_eq_u32 s14, s20\n"
"s_addc_u32 s13, s13, 0\n"
"s_cmp_eq_u32 s14, s22\n"
"s_addc_u32 s13, s13, 0\n"
"s_cmp_eq_u32 s14, s24\n"
"s_addc_u32 s13, s13, 0\n"
"s_mul_i32 s15, s13, 20\n"
"s_add_u32 s12, s12, s15\n"
"s_add_u32 s14, s14, 4\n"
"s_mul_i32 s14, s14, 24\n"
"s_sub_u32 s26, s12, s14\n"
"s_mov_b32 s86, s26\n"
"s_mov_b32 s54, 0\n"
"s_mov_b32 s88, 0x3e0293ee\n"
"s_lshr_b32 s90, s40, 1\n"
"s_and_b32 s91, s40, 1\n"
"v_mbcnt_lo_u32_b32 v100, -1, 0\n"
"v_mbcnt_hi_u32_b32 v100, -1, v100\n"
"v_and_b32 v101, 15, v100\n"
"v_lshrrev_b32 v102, 4, v100\n"
"v_bfe_u32 v103, v100, 2, 2\n"
"v_and_b32 v104, 3, v100\n"
"v_lshlrev_b32 v0, 4, v100\n"
"v_and_b32 v105, 1, v102\n"
"v_lshlrev_b32 v105, 1, v105\n"
"v_xor_b32 v105, v105, v101\n"
"s_and_b32 s44, s41, 1\n"
"s_lshl_b32 s44, s44, 2\n"
"v_xor_b32 v105, s44, v105\n"
"v_lshlrev_b32 v105, 4, v105\n"
"v_lshl_add_u32 v106, v102, 13, v105\n"
"v_xor_b32 v107, 0x80, v106\n"
"v_and_b32 v105, 7, v100\n"
"v_lshrrev_b32 v108, 5, v100\n"
"v_lshlrev_b32 v108, 1, v108\n"
"v_xor_b32 v105, v105, v108\n"
"s_lshr_b32 s44, s41, 1\n"
"s_lshl_b32 s44, s44, 2\n"
"v_xor_b32 v105, s44, v105\n"
"v_lshlrev_b32 v105, 4, v105\n"
"v_lshrrev_b32 v108, 3, v100\n"
"v_lshl_add_u32 v108, v108, 13, v105\n"
"s_lshl_b32 s44, s90, 8\n"
"s_lshl_b32 s45, s41, 15\n"
"s_add_u32 s45, s45, s44\n"
"s_add_u32 s56, s56, s45\n"
"s_addc_u32 s57, s57, 0\n"
"s_add_u32 s58, s58, s45\n"
"s_addc_u32 s59, s59, 0\n"
"s_lshl_b32 s45, s41, 16\n"
"s_add_u32 s45, s45, s44\n"
"s_lshl_b32 s44, s91, 7\n"
"s_add_u32 s45, s45, s44\n"
"s_add_u32 s60, s60, s45\n"
"s_addc_u32 s61, s61, 0\n"
"s_lshl_b32 s66, s41, 10\n"
"s_lshl_b32 s44, s90, 7\n"
"s_lshl_b32 s45, s91, 6\n"
"s_add_u32 s44, s44, s45\n"
"s_lshr_b32 s45, s41, 1\n"
"s_lshl_b32 s45, s45, 5\n"
"s_add_u32 s44, s44, s45\n"
"s_and_b32 s45, s41, 1\n"
"s_lshl_b32 s45, s45, 3\n"
"s_add_u32 s44, s44, s45\n"
"s_lshl_b32 s44, s44, 10\n"
"s_add_u32 s62, s62, s44\n"
"s_addc_u32 s63, s63, 0\n"
"s_lshr_b32 s44, s41, 1\n"
"s_lshl_b32 s44, s44, 7\n"
"v_xor_b32 v109, s44, v0\n"
"s_lshl_b32 s70, s91, 21\n"
"s_lshl_b32 s71, s91, 1\n"
"s_xor_b32 s71, s71, s41\n"
"s_add_u32 s56, s56, s70\n"
"s_addc_u32 s57, s57, 0\n"
"s_add_u32 s58, s58, s70\n"
"s_addc_u32 s59, s59, 0\n"
"s_add_u32 s60, s60, s70\n"
"s_addc_u32 s61, s61, 0\n"
"s_lshl_b32 s70, s70, 1\n"
"v_lshrrev_b32 v111, 2, v101\n"
"v_lshlrev_b32 v111, 1, v111\n"
"v_and_b32 v105, 1, v101\n"
"v_add_u32 v111, v111, v105\n"
"v_lshlrev_b32 v111, 10, v111\n"
"v_lshrrev_b32 v105, 1, v102\n"
"v_lshl_add_u32 v111, v105, 6, v111\n"
"v_and_b32 v105, 1, v102\n"
"v_lshl_add_u32 v111, v105, 4, v111\n"
"v_bfe_u32 v105, v101, 1, 1\n"
"v_lshl_add_u32 v111, v105, 5, v111\n"
"s_lshl_b32 s44, s41, 8\n"
"v_add_u32 v111, s44, v111\n"
"global_load_dwordx4 v[32:35], v111, s[64:65]\n"
"global_load_dwordx4 v[36:39], v111, s[64:65] offset:128\n"
"s_add_u32 m0, s66, 32768\n"
"s_add_u32 s68, s56, 0\n"
"s_addc_u32 s69, s57, 0\n"
"global_load_lds_dwordx4 v106, s[68:69]\n"
"s_add_u32 m0, s66, 36864\n"
"s_add_u32 s68, s56, 131072\n"
"s_addc_u32 s69, s57, 0\n"
"global_load_lds_dwordx4 v107, s[68:69]\n"
"s_add_u32 m0, s66, 40960\n"
"s_add_u32 s68, s56, 262144\n"
"s_addc_u32 s69, s57, 0\n"
"global_load_lds_dwordx4 v106, s[68:69]\n"
"s_add_u32 m0, s66, 45056\n"
"s_add_u32 s68, s56, 393216\n"
"s_addc_u32 s69, s57, 0\n"
"global_load_lds_dwordx4 v107, s[68:69]\n"
"s_add_u32 m0, s66, 49152\n"
"s_add_u32 s68, s58, 0\n"
"s_addc_u32 s69, s59, 0\n"
"global_load_lds_dwordx4 v106, s[68:69]\n"
"s_add_u32 m0, s66, 53248\n"
"s_add_u32 s68, s58, 131072\n"
"s_addc_u32 s69, s59, 0\n"
"global_load_lds_dwordx4 v107, s[68:69]\n"
"s_add_u32 m0, s66, 57344\n"
"s_add_u32 s68, s58, 262144\n"
"s_addc_u32 s69, s59, 0\n"
"global_load_lds_dwordx4 v106, s[68:69]\n"
"s_add_u32 m0, s66, 61440\n"
"s_add_u32 s68, s58, 393216\n"
"s_addc_u32 s69, s59, 0\n"
"global_load_lds_dwordx4 v107, s[68:69]\n"
"s_add_u32 m0, s66, 65536\n"
"s_add_u32 s68, s60, 0\n"
"s_addc_u32 s69, s61, 0\n"
"global_load_lds_dwordx4 v108, s[68:69]\n"
"s_add_u32 m0, s66, 69632\n"
"s_add_u32 s68, s60, 262144\n"
"s_addc_u32 s69, s61, 0\n"
"global_load_lds_dwordx4 v108, s[68:69]\n"
"s_add_u32 m0, s66, 73728\n"
"s_add_u32 s68, s56, 524288\n"
"s_addc_u32 s69, s57, 0\n"
"global_load_lds_dwordx4 v106, s[68:69]\n"
"s_add_u32 m0, s66, 77824\n"
"s_add_u32 s68, s56, 655360\n"
"s_addc_u32 s69, s57, 0\n"
"global_load_lds_dwordx4 v107, s[68:69]\n"
"s_add_u32 m0, s66, 81920\n"
"s_add_u32 s68, s56, 786432\n"
"s_addc_u32 s69, s57, 0\n"
"global_load_lds_dwordx4 v106, s[68:69]\n"
"s_add_u32 m0, s66, 86016\n"
"s_add_u32 s68, s56, 917504\n"
"s_addc_u32 s69, s57, 0\n"
"global_load_lds_dwordx4 v107, s[68:69]\n"
"s_add_u32 m0, s66, 90112\n"
"s_add_u32 s68, s58, 524288\n"
"s_addc_u32 s69, s59, 0\n"
"global_load_lds_dwordx4 v106, s[68:69]\n"
"s_add_u32 m0, s66, 94208\n"
"s_add_u32 s68, s58, 655360\n"
"s_addc_u32 s69, s59, 0\n"
"global_load_lds_dwordx4 v107, s[68:69]\n"
"s_add_u32 m0, s66, 98304\n"
"s_add_u32 s68, s58, 786432\n"
"s_addc_u32 s69, s59, 0\n"
"global_load_lds_dwordx4 v106, s[68:69]\n"
"s_add_u32 m0, s66, 102400\n"
"s_add_u32 s68, s58, 917504\n"
"s_addc_u32 s69, s59, 0\n"
"global_load_lds_dwordx4 v107, s[68:69]\n"
"s_add_u32 m0, s66, 106496\n"
"s_add_u32 s68, s60, 524288\n"
"s_addc_u32 s69, s61, 0\n"
"global_load_lds_dwordx4 v108, s[68:69]\n"
"s_add_u32 m0, s66, 110592\n"
"s_add_u32 s68, s60, 786432\n"
"s_addc_u32 s69, s61, 0\n"
"global_load_lds_dwordx4 v108, s[68:69]\n"
"s_add_u32 m0, s66, 114688\n"
"s_add_u32 s68, s56, 1048576\n"
"s_addc_u32 s69, s57, 0\n"
"global_load_lds_dwordx4 v106, s[68:69]\n"
"s_add_u32 m0, s66, 118784\n"
"s_add_u32 s68, s56, 1179648\n"
"s_addc_u32 s69, s57, 0\n"
"global_load_lds_dwordx4 v107, s[68:69]\n"
"s_add_u32 m0, s66, 122880\n"
"s_add_u32 s68, s56, 1310720\n"
"s_addc_u32 s69, s57, 0\n"
"global_load_lds_dwordx4 v106, s[68:69]\n"
"s_add_u32 m0, s66, 126976\n"
"s_add_u32 s68, s56, 1441792\n"
"s_addc_u32 s69, s57, 0\n"
"global_load_lds_dwordx4 v107, s[68:69]\n"
"s_add_u32 m0, s66, 131072\n"
"s_add_u32 s68, s58, 1048576\n"
"s_addc_u32 s69, s59, 0\n"
"global_load_lds_dwordx4 v106, s[68:69]\n"
"s_add_u32 m0, s66, 135168\n"
"s_add_u32 s68, s58, 1179648\n"
"s_addc_u32 s69, s59, 0\n"
"global_load_lds_dwordx4 v107, s[68:69]\n"
"s_add_u32 m0, s66, 139264\n"
"s_add_u32 s68, s58, 1310720\n"
"s_addc_u32 s69, s59, 0\n"
"global_load_lds_dwordx4 v106, s[68:69]\n"
"s_add_u32 m0, s66, 143360\n"
"s_add_u32 s68, s58, 1441792\n"
"s_addc_u32 s69, s59, 0\n"
"global_load_lds_dwordx4 v107, s[68:69]\n"
"s_add_u32 m0, s66, 147456\n"
"s_add_u32 s68, s60, 1048576\n"
"s_addc_u32 s69, s61, 0\n"
"global_load_lds_dwordx4 v108, s[68:69]\n"
"s_add_u32 m0, s66, 151552\n"
"s_add_u32 s68, s60, 1310720\n"
"s_addc_u32 s69, s61, 0\n"
"global_load_lds_dwordx4 v108, s[68:69]\n"
"s_lshl_b32 s44, s90, 20\n"
"s_lshl_b32 s45, s91, 12\n"
"s_add_u32 s44, s44, s45\n"
"s_mul_i32 s45, s41, 3072\n"
"s_add_u32 s44, s44, s45\n"
"s_add_u32 s46, s46, s44\n"
"s_addc_u32 s47, s47, 0\n"
"s_add_u32 s89, s45, 19456\n"
"s_lshl_b32 s44, s40, 18\n"
"s_lshl_b32 s45, s41, 10\n"
"s_add_u32 s44, s44, s45\n"
"s_add_u32 s50, s50, s44\n"
"s_addc_u32 s51, s51, 0\n"
"s_lshl_b32 s44, s40, 8\n"
"s_add_u32 s52, s52, s44\n"
"s_addc_u32 s53, s53, 0\n"
"s_lshl_b32 s44, s91, 13\n"
"s_add_u32 s48, s44, s45\n"
"s_add_u32 s48, s48, 19456\n"
"s_xor_b32 s44, s91, 1\n"
"s_lshl_b32 s29, s44, 12\n"
"s_lshl_b32 s44, s41, 11\n"
"v_add_u32 v1, s44, v0\n"
"s_mul_i32 s44, s41, 1536\n"
"s_add_u32 s44, s44, 10240\n"
"v_lshlrev_b32 v5, 5, v101\n"
"v_lshrrev_b32 v105, 3, v101\n"
"v_lshl_add_u32 v5, v105, 7, v5\n"
"v_lshl_add_u32 v5, v102, 3, v5\n"
"v_add_u32 v5, s44, v5\n"
"v_mul_u32_u24 v6, 384, v102\n"
"v_lshl_add_u32 v6, v103, 5, v6\n"
"v_lshl_add_u32 v6, v104, 3, v6\n"
"v_add_u32 v6, 10240, v6\n"
"s_lshl_b32 s44, s41, 5\n"
"s_add_u32 s44, s44, 16384\n"
"v_lshrrev_b32 v105, 2, v101\n"
"v_lshlrev_b32 v105, 1, v105\n"
"v_and_b32 v110, 1, v101\n"
"v_add_u32 v105, v105, v110\n"
"v_mul_u32_u24 v8, 144, v105\n"
"v_lshl_add_u32 v7, v102, 3, v8\n"
"v_add_u32 v7, s44, v7\n"
"v_bfe_u32 v110, v101, 1, 1\n"
"v_lshl_add_u32 v105, v110, 2, v102\n"
"v_lshl_add_u32 v8, v105, 4, v8\n"
"v_add_u32 v8, 16384, v8\n"
"v_mul_u32_u24 v9, 0xaaaa, v110\n"
"v_add_u32 v9, 0x4444, v9\n"
"v_lshrrev_b32 v105, 3, v100\n"
"v_lshlrev_b32 v105, 14, v105\n"
"v_and_b32 v110, 7, v100\n"
"v_lshlrev_b32 v110, 4, v110\n"
"s_lshl_b32 s44, s40, 7\n"
"v_add3_u32 v10, v105, v110, s44\n"
"v_bfe_u32 v105, v100, 3, 1\n"
"v_lshrrev_b32 v11, 5, v100\n"
"v_lshl_add_u32 v105, v11, 1, v105\n"
"v_lshlrev_b32 v11, 14, v105\n"
"v_bfe_u32 v105, v100, 4, 1\n"
"v_lshl_add_u32 v11, v105, 3, v11\n"
"v_bfe_u32 v105, v100, 2, 1\n"
"v_lshl_add_u32 v11, v105, 9, v11\n"
"v_lshl_add_u32 v11, v104, 4, v11\n"
"s_lshr_b32 s44, s40, 3\n"
"s_lshl_b32 s44, s44, 10\n"
"s_and_b32 s45, s40, 1\n"
"s_lshl_b32 s45, s45, 8\n"
"s_add_u32 s44, s44, s45\n"
"s_bfe_u32 s45, s40, 0x20001\n"
"s_lshl_b32 s45, s45, 6\n"
"s_add_u32 s44, s44, s45\n"
"v_add_u32 v11, s44, v11\n"
"v_mov_b32 v15, 0x3f803f80\n"
"v_mov_b32 v16, 0x3f803f80\n"
"v_mov_b32 v17, 0x3f803f80\n"
"v_mov_b32 v18, 0x3f803f80\n"
"v_mov_b32 v19, 0x3f803f80\n"
"v_mov_b32 v21, 0\n"
"v_mov_b32 v22, 0\n"
"v_mov_b32 v23, 0\n"
"v_lshrrev_b32 v105, 1, v102\n"
"v_lshlrev_b32 v105, 3, v105\n"
"v_and_b32 v110, 1, v102\n"
"v_lshl_add_u32 v105, v110, 2, v105\n"
"v_lshrrev_b32 v111, 1, v103\n"
"v_lshl_add_u32 v105, v111, 4, v105\n"
"v_and_b32 v116, 1, v103\n"
"v_add_u32 v105, v105, v116\n"
"v_lshl_add_u32 v117, v110, 1, v116\n"
"v_lshl_add_u32 v117, v111, 2, v117\n"
"v_lshrrev_b32 v118, 1, v104\n"
"s_lshl_b32 s44, s41, 2\n"
"v_add_u32 v119, s44, v118\n"
"v_lshlrev_b32 v120, 1, v117\n"
"v_xor_b32 v119, v119, v120\n"
"v_lshlrev_b32 v119, 4, v119\n"
"v_and_b32 v121, 1, v104\n"
"v_lshl_add_u32 v119, v121, 3, v119\n"
"v_lshl_add_u32 v112, v105, 8, v119\n"
"v_xor_b32 v113, 32, v112\n"
"s_lshl_b32 s44, s41, 1\n"
"v_add_u32 v119, s44, v118\n"
"v_lshrrev_b32 v120, 1, v117\n"
"v_lshlrev_b32 v120, 1, v120\n"
"v_xor_b32 v119, v119, v120\n"
"v_lshlrev_b32 v119, 4, v119\n"
"v_lshl_add_u32 v119, v121, 3, v119\n"
"v_lshl_add_u32 v114, v105, 7, v119\n"
"v_lshlrev_b32 v105, 4, v111\n"
"v_lshl_add_u32 v105, v102, 2, v105\n"
"v_add_u32 v105, v105, v116\n"
"s_lshl_b32 s44, s41, 4\n"
"v_add_u32 v119, s44, v118\n"
"v_lshlrev_b32 v120, 1, v117\n"
"v_xor_b32 v119, v119, v120\n"
"v_lshlrev_b32 v119, 4, v119\n"
"v_lshl_add_u32 v119, v121, 3, v119\n"
"v_lshl_add_u32 v115, v105, 10, v119\n"
"s_waitcnt vmcnt(20)\n"
"s_barrier\n"
"v_add_u32 v122, 32768, v112\n"
"v_add_u32 v123, 32768, v113\n"
"v_add_u32 v124, 65536, v114\n"
"s_cmp_eq_u32 s71, 0\n"
"s_cbranch_scc0 CH_PW0_0\n"
"ds_read_b64_tr_b16 a[0:1], v122 offset:0\n"
"ds_read_b64_tr_b16 a[2:3], v122 offset:512\n"
"ds_read_b64_tr_b16 a[4:5], v122 offset:8192\n"
"ds_read_b64_tr_b16 a[6:7], v122 offset:8704\n"
"ds_read_b64_tr_b16 a[64:65], v123 offset:0\n"
"ds_read_b64_tr_b16 a[66:67], v123 offset:512\n"
"ds_read_b64_tr_b16 a[68:69], v123 offset:8192\n"
"ds_read_b64_tr_b16 a[70:71], v123 offset:8704\n"
"s_waitcnt lgkmcnt(0)\n"
"ds_read_b64_tr_b16 a[128:129], v122 offset:16384\n"
"ds_read_b64_tr_b16 a[130:131], v122 offset:16896\n"
"ds_read_b64_tr_b16 a[132:133], v122 offset:24576\n"
"ds_read_b64_tr_b16 a[134:135], v122 offset:25088\n"
"ds_read_b64_tr_b16 a[192:193], v123 offset:16384\n"
"ds_read_b64_tr_b16 a[194:195], v123 offset:16896\n"
"ds_read_b64_tr_b16 a[196:197], v123 offset:24576\n"
"ds_read_b64_tr_b16 a[198:199], v123 offset:25088\n"
"s_waitcnt lgkmcnt(0)\n"
"ds_read_b64_tr_b16 v[128:129], v124 offset:0\n"
"ds_read_b64_tr_b16 v[130:131], v124 offset:256\n"
"ds_read_b64_tr_b16 v[132:133], v124 offset:4096\n"
"ds_read_b64_tr_b16 v[134:135], v124 offset:4352\n"
"s_branch CH_PE0\n"
"CH_PW0_0:\n"
"s_cmp_eq_u32 s71, 1\n"
"s_cbranch_scc0 CH_PW0_1\n"
"ds_read_b64_tr_b16 a[16:17], v122 offset:0\n"
"ds_read_b64_tr_b16 a[18:19], v122 offset:512\n"
"ds_read_b64_tr_b16 a[20:21], v122 offset:8192\n"
"ds_read_b64_tr_b16 a[22:23], v122 offset:8704\n"
"ds_read_b64_tr_b16 a[80:81], v123 offset:0\n"
"ds_read_b64_tr_b16 a[82:83], v123 offset:512\n"
"ds_read_b64_tr_b16 a[84:85], v123 offset:8192\n"
"ds_read_b64_tr_b16 a[86:87], v123 offset:8704\n"
"s_waitcnt lgkmcnt(0)\n"
"ds_read_b64_tr_b16 a[144:145], v122 offset:16384\n"
"ds_read_b64_tr_b16 a[146:147], v122 offset:16896\n"
"ds_read_b64_tr_b16 a[148:149], v122 offset:24576\n"
"ds_read_b64_tr_b16 a[150:151], v122 offset:25088\n"
"ds_read_b64_tr_b16 a[208:209], v123 offset:16384\n"
"ds_read_b64_tr_b16 a[210:211], v123 offset:16896\n"
"ds_read_b64_tr_b16 a[212:213], v123 offset:24576\n"
"ds_read_b64_tr_b16 a[214:215], v123 offset:25088\n"
"s_waitcnt lgkmcnt(0)\n"
"ds_read_b64_tr_b16 v[144:145], v124 offset:0\n"
"ds_read_b64_tr_b16 v[146:147], v124 offset:256\n"
"ds_read_b64_tr_b16 v[148:149], v124 offset:4096\n"
"ds_read_b64_tr_b16 v[150:151], v124 offset:4352\n"
"s_branch CH_PE0\n"
"CH_PW0_1:\n"
"s_cmp_eq_u32 s71, 2\n"
"s_cbranch_scc0 CH_PW0_2\n"
"ds_read_b64_tr_b16 a[32:33], v122 offset:0\n"
"ds_read_b64_tr_b16 a[34:35], v122 offset:512\n"
"ds_read_b64_tr_b16 a[36:37], v122 offset:8192\n"
"ds_read_b64_tr_b16 a[38:39], v122 offset:8704\n"
"ds_read_b64_tr_b16 a[96:97], v123 offset:0\n"
"ds_read_b64_tr_b16 a[98:99], v123 offset:512\n"
"ds_read_b64_tr_b16 a[100:101], v123 offset:8192\n"
"ds_read_b64_tr_b16 a[102:103], v123 offset:8704\n"
"s_waitcnt lgkmcnt(0)\n"
"ds_read_b64_tr_b16 a[160:161], v122 offset:16384\n"
"ds_read_b64_tr_b16 a[162:163], v122 offset:16896\n"
"ds_read_b64_tr_b16 a[164:165], v122 offset:24576\n"
"ds_read_b64_tr_b16 a[166:167], v122 offset:25088\n"
"ds_read_b64_tr_b16 a[224:225], v123 offset:16384\n"
"ds_read_b64_tr_b16 a[226:227], v123 offset:16896\n"
"ds_read_b64_tr_b16 a[228:229], v123 offset:24576\n"
"ds_read_b64_tr_b16 a[230:231], v123 offset:25088\n"
"s_waitcnt lgkmcnt(0)\n"
"ds_read_b64_tr_b16 v[160:161], v124 offset:0\n"
"ds_read_b64_tr_b16 v[162:163], v124 offset:256\n"
"ds_read_b64_tr_b16 v[164:165], v124 offset:4096\n"
"ds_read_b64_tr_b16 v[166:167], v124 offset:4352\n"
"s_branch CH_PE0\n"
"CH_PW0_2:\n"
"ds_read_b64_tr_b16 a[48:49], v122 offset:0\n"
"ds_read_b64_tr_b16 a[50:51], v122 offset:512\n"
"ds_read_b64_tr_b16 a[52:53], v122 offset:8192\n"
"ds_read_b64_tr_b16 a[54:55], v122 offset:8704\n"
"ds_read_b64_tr_b16 a[112:113], v123 offset:0\n"
"ds_read_b64_tr_b16 a[114:115], v123 offset:512\n"
"ds_read_b64_tr_b16 a[116:117], v123 offset:8192\n"
"ds_read_b64_tr_b16 a[118:119], v123 offset:8704\n"
"s_waitcnt lgkmcnt(0)\n"
"ds_read_b64_tr_b16 a[176:177], v122 offset:16384\n"
"ds_read_b64_tr_b16 a[178:179], v122 offset:16896\n"
"ds_read_b64_tr_b16 a[180:181], v122 offset:24576\n"
"ds_read_b64_tr_b16 a[182:183], v122 offset:25088\n"
"ds_read_b64_tr_b16 a[240:241], v123 offset:16384\n"
"ds_read_b64_tr_b16 a[242:243], v123 offset:16896\n"
"ds_read_b64_tr_b16 a[244:245], v123 offset:24576\n"
"ds_read_b64_tr_b16 a[246:247], v123 offset:25088\n"
"s_waitcnt lgkmcnt(0)\n"
"ds_read_b64_tr_b16 v[176:177], v124 offset:0\n"
"ds_read_b64_tr_b16 v[178:179], v124 offset:256\n"
"ds_read_b64_tr_b16 v[180:181], v124 offset:4096\n"
"ds_read_b64_tr_b16 v[182:183], v124 offset:4352\n"
"CH_PE0:\n"
"s_waitcnt lgkmcnt(0)\n"
"s_barrier\n"
"s_add_u32 m0, s66, 32768\n"
"s_add_u32 s68, s56, 1572864\n"
"s_addc_u32 s69, s57, 0\n"
"global_load_lds_dwordx4 v106, s[68:69]\n"
"s_add_u32 m0, s66, 36864\n"
"s_add_u32 s68, s56, 1703936\n"
"s_addc_u32 s69, s57, 0\n"
"global_load_lds_dwordx4 v107, s[68:69]\n"
"s_add_u32 m0, s66, 40960\n"
"s_add_u32 s68, s56, 1835008\n"
"s_addc_u32 s69, s57, 0\n"
"global_load_lds_dwordx4 v106, s[68:69]\n"
"s_add_u32 m0, s66, 45056\n"
"s_add_u32 s68, s56, 1966080\n"
"s_addc_u32 s69, s57, 0\n"
"global_load_lds_dwordx4 v107, s[68:69]\n"
"s_add_u32 m0, s66, 49152\n"
"s_add_u32 s68, s58, 1572864\n"
"s_addc_u32 s69, s59, 0\n"
"global_load_lds_dwordx4 v106, s[68:69]\n"
"s_add_u32 m0, s66, 53248\n"
"s_add_u32 s68, s58, 1703936\n"
"s_addc_u32 s69, s59, 0\n"
"global_load_lds_dwordx4 v107, s[68:69]\n"
"s_add_u32 m0, s66, 57344\n"
"s_add_u32 s68, s58, 1835008\n"
"s_addc_u32 s69, s59, 0\n"
"global_load_lds_dwordx4 v106, s[68:69]\n"
"s_add_u32 m0, s66, 61440\n"
"s_add_u32 s68, s58, 1966080\n"
"s_addc_u32 s69, s59, 0\n"
"global_load_lds_dwordx4 v107, s[68:69]\n"
"s_add_u32 m0, s66, 65536\n"
"s_add_u32 s68, s60, 1572864\n"
"s_addc_u32 s69, s61, 0\n"
"global_load_lds_dwordx4 v108, s[68:69]\n"
"s_add_u32 m0, s66, 69632\n"
"s_add_u32 s68, s60, 1835008\n"
"s_addc_u32 s69, s61, 0\n"
"global_load_lds_dwordx4 v108, s[68:69]\n"
"s_waitcnt vmcnt(20)\n"
"s_barrier\n"
"v_add_u32 v122, 73728, v112\n"
"v_add_u32 v123, 73728, v113\n"
"v_add_u32 v124, 106496, v114\n"
"s_cmp_eq_u32 s71, 0\n"
"s_cbranch_scc0 CH_PW1_0\n"
"ds_read_b64_tr_b16 a[8:9], v122 offset:0\n"
"ds_read_b64_tr_b16 a[10:11], v122 offset:512\n"
"ds_read_b64_tr_b16 a[12:13], v122 offset:8192\n"
"ds_read_b64_tr_b16 a[14:15], v122 offset:8704\n"
"ds_read_b64_tr_b16 a[72:73], v123 offset:0\n"
"ds_read_b64_tr_b16 a[74:75], v123 offset:512\n"
"ds_read_b64_tr_b16 a[76:77], v123 offset:8192\n"
"ds_read_b64_tr_b16 a[78:79], v123 offset:8704\n"
"s_waitcnt lgkmcnt(0)\n"
"ds_read_b64_tr_b16 a[136:137], v122 offset:16384\n"
"ds_read_b64_tr_b16 a[138:139], v122 offset:16896\n"
"ds_read_b64_tr_b16 a[140:141], v122 offset:24576\n"
"ds_read_b64_tr_b16 a[142:143], v122 offset:25088\n"
"ds_read_b64_tr_b16 a[200:201], v123 offset:16384\n"
"ds_read_b64_tr_b16 a[202:203], v123 offset:16896\n"
"ds_read_b64_tr_b16 a[204:205], v123 offset:24576\n"
"ds_read_b64_tr_b16 a[206:207], v123 offset:25088\n"
"s_waitcnt lgkmcnt(0)\n"
"ds_read_b64_tr_b16 v[136:137], v124 offset:0\n"
"ds_read_b64_tr_b16 v[138:139], v124 offset:256\n"
"ds_read_b64_tr_b16 v[140:141], v124 offset:4096\n"
"ds_read_b64_tr_b16 v[142:143], v124 offset:4352\n"
"s_branch CH_PE1\n"
"CH_PW1_0:\n"
"s_cmp_eq_u32 s71, 1\n"
"s_cbranch_scc0 CH_PW1_1\n"
"ds_read_b64_tr_b16 a[24:25], v122 offset:0\n"
"ds_read_b64_tr_b16 a[26:27], v122 offset:512\n"
"ds_read_b64_tr_b16 a[28:29], v122 offset:8192\n"
"ds_read_b64_tr_b16 a[30:31], v122 offset:8704\n"
"ds_read_b64_tr_b16 a[88:89], v123 offset:0\n"
"ds_read_b64_tr_b16 a[90:91], v123 offset:512\n"
"ds_read_b64_tr_b16 a[92:93], v123 offset:8192\n"
"ds_read_b64_tr_b16 a[94:95], v123 offset:8704\n"
"s_waitcnt lgkmcnt(0)\n"
"ds_read_b64_tr_b16 a[152:153], v122 offset:16384\n"
"ds_read_b64_tr_b16 a[154:155], v122 offset:16896\n"
"ds_read_b64_tr_b16 a[156:157], v122 offset:24576\n"
"ds_read_b64_tr_b16 a[158:159], v122 offset:25088\n"
"ds_read_b64_tr_b16 a[216:217], v123 offset:16384\n"
"ds_read_b64_tr_b16 a[218:219], v123 offset:16896\n"
"ds_read_b64_tr_b16 a[220:221], v123 offset:24576\n"
"ds_read_b64_tr_b16 a[222:223], v123 offset:25088\n"
"s_waitcnt lgkmcnt(0)\n"
"ds_read_b64_tr_b16 v[152:153], v124 offset:0\n"
"ds_read_b64_tr_b16 v[154:155], v124 offset:256\n"
"ds_read_b64_tr_b16 v[156:157], v124 offset:4096\n"
"ds_read_b64_tr_b16 v[158:159], v124 offset:4352\n"
"s_branch CH_PE1\n"
"CH_PW1_1:\n"
"s_cmp_eq_u32 s71, 2\n"
"s_cbranch_scc0 CH_PW1_2\n"
"ds_read_b64_tr_b16 a[40:41], v122 offset:0\n"
"ds_read_b64_tr_b16 a[42:43], v122 offset:512\n"
"ds_read_b64_tr_b16 a[44:45], v122 offset:8192\n"
"ds_read_b64_tr_b16 a[46:47], v122 offset:8704\n"
"ds_read_b64_tr_b16 a[104:105], v123 offset:0\n"
"ds_read_b64_tr_b16 a[106:107], v123 offset:512\n"
"ds_read_b64_tr_b16 a[108:109], v123 offset:8192\n"
"ds_read_b64_tr_b16 a[110:111], v123 offset:8704\n"
"s_waitcnt lgkmcnt(0)\n"
"ds_read_b64_tr_b16 a[168:169], v122 offset:16384\n"
"ds_read_b64_tr_b16 a[170:171], v122 offset:16896\n"
"ds_read_b64_tr_b16 a[172:173], v122 offset:24576\n"
"ds_read_b64_tr_b16 a[174:175], v122 offset:25088\n"
"ds_read_b64_tr_b16 a[232:233], v123 offset:16384\n"
"ds_read_b64_tr_b16 a[234:235], v123 offset:16896\n"
"ds_read_b64_tr_b16 a[236:237], v123 offset:24576\n"
"ds_read_b64_tr_b16 a[238:239], v123 offset:25088\n"
"s_waitcnt lgkmcnt(0)\n"
"ds_read_b64_tr_b16 v[168:169], v124 offset:0\n"
"ds_read_b64_tr_b16 v[170:171], v124 offset:256\n"
"ds_read_b64_tr_b16 v[172:173], v124 offset:4096\n"
"ds_read_b64_tr_b16 v[174:175], v124 offset:4352\n"
"s_branch CH_PE1\n"
"CH_PW1_2:\n"
"ds_read_b64_tr_b16 a[56:57], v122 offset:0\n"
"ds_read_b64_tr_b16 a[58:59], v122 offset:512\n"
"ds_read_b64_tr_b16 a[60:61], v122 offset:8192\n"
"ds_read_b64_tr_b16 a[62:63], v122 offset:8704\n"
"ds_read_b64_tr_b16 a[120:121], v123 offset:0\n"
"ds_read_b64_tr_b16 a[122:123], v123 offset:512\n"
"ds_read_b64_tr_b16 a[124:125], v123 offset:8192\n"
"ds_read_b64_tr_b16 a[126:127], v123 offset:8704\n"
"s_waitcnt lgkmcnt(0)\n"
"ds_read_b64_tr_b16 a[184:185], v122 offset:16384\n"
"ds_read_b64_tr_b16 a[186:187], v122 offset:16896\n"
"ds_read_b64_tr_b16 a[188:189], v122 offset:24576\n"
"ds_read_b64_tr_b16 a[190:191], v122 offset:25088\n"
"ds_read_b64_tr_b16 a[248:249], v123 offset:16384\n"
"ds_read_b64_tr_b16 a[250:251], v123 offset:16896\n"
"ds_read_b64_tr_b16 a[252:253], v123 offset:24576\n"
"ds_read_b64_tr_b16 a[254:255], v123 offset:25088\n"
"s_waitcnt lgkmcnt(0)\n"
"ds_read_b64_tr_b16 v[184:185], v124 offset:0\n"
"ds_read_b64_tr_b16 v[186:187], v124 offset:256\n"
"ds_read_b64_tr_b16 v[188:189], v124 offset:4096\n"
"ds_read_b64_tr_b16 v[190:191], v124 offset:4352\n"
"CH_PE1:\n"
"s_waitcnt lgkmcnt(0)\n"
"s_barrier\n"
"s_sub_u32 s56, s56, s70\n"
"s_subb_u32 s57, s57, 0\n"
"s_sub_u32 s58, s58, s70\n"
"s_subb_u32 s59, s59, 0\n"
"s_sub_u32 s60, s60, s70\n"
"s_subb_u32 s61, s61, 0\n"
"s_add_u32 m0, s66, 73728\n"
"s_add_u32 s68, s56, 2097152\n"
"s_addc_u32 s69, s57, 0\n"
"global_load_lds_dwordx4 v106, s[68:69]\n"
"s_add_u32 m0, s66, 77824\n"
"s_add_u32 s68, s56, 2228224\n"
"s_addc_u32 s69, s57, 0\n"
"global_load_lds_dwordx4 v107, s[68:69]\n"
"s_add_u32 m0, s66, 81920\n"
"s_add_u32 s68, s56, 2359296\n"
"s_addc_u32 s69, s57, 0\n"
"global_load_lds_dwordx4 v106, s[68:69]\n"
"s_add_u32 m0, s66, 86016\n"
"s_add_u32 s68, s56, 2490368\n"
"s_addc_u32 s69, s57, 0\n"
"global_load_lds_dwordx4 v107, s[68:69]\n"
"s_add_u32 m0, s66, 90112\n"
"s_add_u32 s68, s58, 2097152\n"
"s_addc_u32 s69, s59, 0\n"
"global_load_lds_dwordx4 v106, s[68:69]\n"
"s_add_u32 m0, s66, 94208\n"
"s_add_u32 s68, s58, 2228224\n"
"s_addc_u32 s69, s59, 0\n"
"global_load_lds_dwordx4 v107, s[68:69]\n"
"s_add_u32 m0, s66, 98304\n"
"s_add_u32 s68, s58, 2359296\n"
"s_addc_u32 s69, s59, 0\n"
"global_load_lds_dwordx4 v106, s[68:69]\n"
"s_add_u32 m0, s66, 102400\n"
"s_add_u32 s68, s58, 2490368\n"
"s_addc_u32 s69, s59, 0\n"
"global_load_lds_dwordx4 v107, s[68:69]\n"
"s_add_u32 m0, s66, 106496\n"
"s_add_u32 s68, s60, 2097152\n"
"s_addc_u32 s69, s61, 0\n"
"global_load_lds_dwordx4 v108, s[68:69]\n"
"s_add_u32 m0, s66, 110592\n"
"s_add_u32 s68, s60, 2359296\n"
"s_addc_u32 s69, s61, 0\n"
"global_load_lds_dwordx4 v108, s[68:69]\n"
"s_waitcnt vmcnt(20)\n"
"s_barrier\n"
"v_add_u32 v122, 114688, v112\n"
"v_add_u32 v123, 114688, v113\n"
"v_add_u32 v124, 147456, v114\n"
"s_cmp_eq_u32 s71, 0\n"
"s_cbranch_scc0 CH_PW2_0\n"
"ds_read_b64_tr_b16 a[16:17], v122 offset:0\n"
"ds_read_b64_tr_b16 a[18:19], v122 offset:512\n"
"ds_read_b64_tr_b16 a[20:21], v122 offset:8192\n"
"ds_read_b64_tr_b16 a[22:23], v122 offset:8704\n"
"ds_read_b64_tr_b16 a[80:81], v123 offset:0\n"
"ds_read_b64_tr_b16 a[82:83], v123 offset:512\n"
"ds_read_b64_tr_b16 a[84:85], v123 offset:8192\n"
"ds_read_b64_tr_b16 a[86:87], v123 offset:8704\n"
"s_waitcnt lgkmcnt(0)\n"
"ds_read_b64_tr_b16 a[144:145], v122 offset:16384\n"
"ds_read_b64_tr_b16 a[146:147], v122 offset:16896\n"
"ds_read_b64_tr_b16 a[148:149], v122 offset:24576\n"
"ds_read_b64_tr_b16 a[150:151], v122 offset:25088\n"
"ds_read_b64_tr_b16 a[208:209], v123 offset:16384\n"
"ds_read_b64_tr_b16 a[210:211], v123 offset:16896\n"
"ds_read_b64_tr_b16 a[212:213], v123 offset:24576\n"
"ds_read_b64_tr_b16 a[214:215], v123 offset:25088\n"
"s_waitcnt lgkmcnt(0)\n"
"ds_read_b64_tr_b16 v[144:145], v124 offset:0\n"
"ds_read_b64_tr_b16 v[146:147], v124 offset:256\n"
"ds_read_b64_tr_b16 v[148:149], v124 offset:4096\n"
"ds_read_b64_tr_b16 v[150:151], v124 offset:4352\n"
"s_branch CH_PE2\n"
"CH_PW2_0:\n"
"s_cmp_eq_u32 s71, 1\n"
"s_cbranch_scc0 CH_PW2_1\n"
"ds_read_b64_tr_b16 a[0:1], v122 offset:0\n"
"ds_read_b64_tr_b16 a[2:3], v122 offset:512\n"
"ds_read_b64_tr_b16 a[4:5], v122 offset:8192\n"
"ds_read_b64_tr_b16 a[6:7], v122 offset:8704\n"
"ds_read_b64_tr_b16 a[64:65], v123 offset:0\n"
"ds_read_b64_tr_b16 a[66:67], v123 offset:512\n"
"ds_read_b64_tr_b16 a[68:69], v123 offset:8192\n"
"ds_read_b64_tr_b16 a[70:71], v123 offset:8704\n"
"s_waitcnt lgkmcnt(0)\n"
"ds_read_b64_tr_b16 a[128:129], v122 offset:16384\n"
"ds_read_b64_tr_b16 a[130:131], v122 offset:16896\n"
"ds_read_b64_tr_b16 a[132:133], v122 offset:24576\n"
"ds_read_b64_tr_b16 a[134:135], v122 offset:25088\n"
"ds_read_b64_tr_b16 a[192:193], v123 offset:16384\n"
"ds_read_b64_tr_b16 a[194:195], v123 offset:16896\n"
"ds_read_b64_tr_b16 a[196:197], v123 offset:24576\n"
"ds_read_b64_tr_b16 a[198:199], v123 offset:25088\n"
"s_waitcnt lgkmcnt(0)\n"
"ds_read_b64_tr_b16 v[128:129], v124 offset:0\n"
"ds_read_b64_tr_b16 v[130:131], v124 offset:256\n"
"ds_read_b64_tr_b16 v[132:133], v124 offset:4096\n"
"ds_read_b64_tr_b16 v[134:135], v124 offset:4352\n"
"s_branch CH_PE2\n"
"CH_PW2_1:\n"
"s_cmp_eq_u32 s71, 2\n"
"s_cbranch_scc0 CH_PW2_2\n"
"ds_read_b64_tr_b16 a[48:49], v122 offset:0\n"
"ds_read_b64_tr_b16 a[50:51], v122 offset:512\n"
"ds_read_b64_tr_b16 a[52:53], v122 offset:8192\n"
"ds_read_b64_tr_b16 a[54:55], v122 offset:8704\n"
"ds_read_b64_tr_b16 a[112:113], v123 offset:0\n"
"ds_read_b64_tr_b16 a[114:115], v123 offset:512\n"
"ds_read_b64_tr_b16 a[116:117], v123 offset:8192\n"
"ds_read_b64_tr_b16 a[118:119], v123 offset:8704\n"
"s_waitcnt lgkmcnt(0)\n"
"ds_read_b64_tr_b16 a[176:177], v122 offset:16384\n"
"ds_read_b64_tr_b16 a[178:179], v122 offset:16896\n"
"ds_read_b64_tr_b16 a[180:181], v122 offset:24576\n"
"ds_read_b64_tr_b16 a[182:183], v122 offset:25088\n"
"ds_read_b64_tr_b16 a[240:241], v123 offset:16384\n"
"ds_read_b64_tr_b16 a[242:243], v123 offset:16896\n"
"ds_read_b64_tr_b16 a[244:245], v123 offset:24576\n"
"ds_read_b64_tr_b16 a[246:247], v123 offset:25088\n"
"s_waitcnt lgkmcnt(0)\n"
"ds_read_b64_tr_b16 v[176:177], v124 offset:0\n"
"ds_read_b64_tr_b16 v[178:179], v124 offset:256\n"
"ds_read_b64_tr_b16 v[180:181], v124 offset:4096\n"
"ds_read_b64_tr_b16 v[182:183], v124 offset:4352\n"
"s_branch CH_PE2\n"
"CH_PW2_2:\n"
"ds_read_b64_tr_b16 a[32:33], v122 offset:0\n"
"ds_read_b64_tr_b16 a[34:35], v122 offset:512\n"
"ds_read_b64_tr_b16 a[36:37], v122 offset:8192\n"
"ds_read_b64_tr_b16 a[38:39], v122 offset:8704\n"
"ds_read_b64_tr_b16 a[96:97], v123 offset:0\n"
"ds_read_b64_tr_b16 a[98:99], v123 offset:512\n"
"ds_read_b64_tr_b16 a[100:101], v123 offset:8192\n"
"ds_read_b64_tr_b16 a[102:103], v123 offset:8704\n"
"s_waitcnt lgkmcnt(0)\n"
"ds_read_b64_tr_b16 a[160:161], v122 offset:16384\n"
"ds_read_b64_tr_b16 a[162:163], v122 offset:16896\n"
"ds_read_b64_tr_b16 a[164:165], v122 offset:24576\n"
"ds_read_b64_tr_b16 a[166:167], v122 offset:25088\n"
"ds_read_b64_tr_b16 a[224:225], v123 offset:16384\n"
"ds_read_b64_tr_b16 a[226:227], v123 offset:16896\n"
"ds_read_b64_tr_b16 a[228:229], v123 offset:24576\n"
"ds_read_b64_tr_b16 a[230:231], v123 offset:25088\n"
"s_waitcnt lgkmcnt(0)\n"
"ds_read_b64_tr_b16 v[160:161], v124 offset:0\n"
"ds_read_b64_tr_b16 v[162:163], v124 offset:256\n"
"ds_read_b64_tr_b16 v[164:165], v124 offset:4096\n"
"ds_read_b64_tr_b16 v[166:167], v124 offset:4352\n"
"CH_PE2:\n"
"s_waitcnt lgkmcnt(0)\n"
"s_barrier\n"
"s_add_u32 m0, s66, 114688\n"
"s_add_u32 s68, s56, 2621440\n"
"s_addc_u32 s69, s57, 0\n"
"global_load_lds_dwordx4 v106, s[68:69]\n"
"s_add_u32 m0, s66, 118784\n"
"s_add_u32 s68, s56, 2752512\n"
"s_addc_u32 s69, s57, 0\n"
"global_load_lds_dwordx4 v107, s[68:69]\n"
"s_add_u32 m0, s66, 122880\n"
"s_add_u32 s68, s56, 2883584\n"
"s_addc_u32 s69, s57, 0\n"
"global_load_lds_dwordx4 v106, s[68:69]\n"
"s_add_u32 m0, s66, 126976\n"
"s_add_u32 s68, s56, 3014656\n"
"s_addc_u32 s69, s57, 0\n"
"global_load_lds_dwordx4 v107, s[68:69]\n"
"s_add_u32 m0, s66, 131072\n"
"s_add_u32 s68, s58, 2621440\n"
"s_addc_u32 s69, s59, 0\n"
"global_load_lds_dwordx4 v106, s[68:69]\n"
"s_add_u32 m0, s66, 135168\n"
"s_add_u32 s68, s58, 2752512\n"
"s_addc_u32 s69, s59, 0\n"
"global_load_lds_dwordx4 v107, s[68:69]\n"
"s_add_u32 m0, s66, 139264\n"
"s_add_u32 s68, s58, 2883584\n"
"s_addc_u32 s69, s59, 0\n"
"global_load_lds_dwordx4 v106, s[68:69]\n"
"s_add_u32 m0, s66, 143360\n"
"s_add_u32 s68, s58, 3014656\n"
"s_addc_u32 s69, s59, 0\n"
"global_load_lds_dwordx4 v107, s[68:69]\n"
"s_add_u32 m0, s66, 147456\n"
"s_add_u32 s68, s60, 2621440\n"
"s_addc_u32 s69, s61, 0\n"
"global_load_lds_dwordx4 v108, s[68:69]\n"
"s_add_u32 m0, s66, 151552\n"
"s_add_u32 s68, s60, 2883584\n"
"s_addc_u32 s69, s61, 0\n"
"global_load_lds_dwordx4 v108, s[68:69]\n"
"s_waitcnt vmcnt(20)\n"
"s_barrier\n"
"v_add_u32 v122, 32768, v112\n"
"v_add_u32 v123, 32768, v113\n"
"v_add_u32 v124, 65536, v114\n"
"s_cmp_eq_u32 s71, 0\n"
"s_cbranch_scc0 CH_PW3_0\n"
"ds_read_b64_tr_b16 a[24:25], v122 offset:0\n"
"ds_read_b64_tr_b16 a[26:27], v122 offset:512\n"
"ds_read_b64_tr_b16 a[28:29], v122 offset:8192\n"
"ds_read_b64_tr_b16 a[30:31], v122 offset:8704\n"
"ds_read_b64_tr_b16 a[88:89], v123 offset:0\n"
"ds_read_b64_tr_b16 a[90:91], v123 offset:512\n"
"ds_read_b64_tr_b16 a[92:93], v123 offset:8192\n"
"ds_read_b64_tr_b16 a[94:95], v123 offset:8704\n"
"s_waitcnt lgkmcnt(0)\n"
"ds_read_b64_tr_b16 a[152:153], v122 offset:16384\n"
"ds_read_b64_tr_b16 a[154:155], v122 offset:16896\n"
"ds_read_b64_tr_b16 a[156:157], v122 offset:24576\n"
"ds_read_b64_tr_b16 a[158:159], v122 offset:25088\n"
"ds_read_b64_tr_b16 a[216:217], v123 offset:16384\n"
"ds_read_b64_tr_b16 a[218:219], v123 offset:16896\n"
"ds_read_b64_tr_b16 a[220:221], v123 offset:24576\n"
"ds_read_b64_tr_b16 a[222:223], v123 offset:25088\n"
"s_waitcnt lgkmcnt(0)\n"
"ds_read_b64_tr_b16 v[152:153], v124 offset:0\n"
"ds_read_b64_tr_b16 v[154:155], v124 offset:256\n"
"ds_read_b64_tr_b16 v[156:157], v124 offset:4096\n"
"ds_read_b64_tr_b16 v[158:159], v124 offset:4352\n"
"s_branch CH_PE3\n"
"CH_PW3_0:\n"
"s_cmp_eq_u32 s71, 1\n"
"s_cbranch_scc0 CH_PW3_1\n"
"ds_read_b64_tr_b16 a[8:9], v122 offset:0\n"
"ds_read_b64_tr_b16 a[10:11], v122 offset:512\n"
"ds_read_b64_tr_b16 a[12:13], v122 offset:8192\n"
"ds_read_b64_tr_b16 a[14:15], v122 offset:8704\n"
"ds_read_b64_tr_b16 a[72:73], v123 offset:0\n"
"ds_read_b64_tr_b16 a[74:75], v123 offset:512\n"
"ds_read_b64_tr_b16 a[76:77], v123 offset:8192\n"
"ds_read_b64_tr_b16 a[78:79], v123 offset:8704\n"
"s_waitcnt lgkmcnt(0)\n"
"ds_read_b64_tr_b16 a[136:137], v122 offset:16384\n"
"ds_read_b64_tr_b16 a[138:139], v122 offset:16896\n"
"ds_read_b64_tr_b16 a[140:141], v122 offset:24576\n"
"ds_read_b64_tr_b16 a[142:143], v122 offset:25088\n"
"ds_read_b64_tr_b16 a[200:201], v123 offset:16384\n"
"ds_read_b64_tr_b16 a[202:203], v123 offset:16896\n"
"ds_read_b64_tr_b16 a[204:205], v123 offset:24576\n"
"ds_read_b64_tr_b16 a[206:207], v123 offset:25088\n"
"s_waitcnt lgkmcnt(0)\n"
"ds_read_b64_tr_b16 v[136:137], v124 offset:0\n"
"ds_read_b64_tr_b16 v[138:139], v124 offset:256\n"
"ds_read_b64_tr_b16 v[140:141], v124 offset:4096\n"
"ds_read_b64_tr_b16 v[142:143], v124 offset:4352\n"
"s_branch CH_PE3\n"
"CH_PW3_1:\n"
"s_cmp_eq_u32 s71, 2\n"
"s_cbranch_scc0 CH_PW3_2\n"
"ds_read_b64_tr_b16 a[56:57], v122 offset:0\n"
"ds_read_b64_tr_b16 a[58:59], v122 offset:512\n"
"ds_read_b64_tr_b16 a[60:61], v122 offset:8192\n"
"ds_read_b64_tr_b16 a[62:63], v122 offset:8704\n"
"ds_read_b64_tr_b16 a[120:121], v123 offset:0\n"
"ds_read_b64_tr_b16 a[122:123], v123 offset:512\n"
"ds_read_b64_tr_b16 a[124:125], v123 offset:8192\n"
"ds_read_b64_tr_b16 a[126:127], v123 offset:8704\n"
"s_waitcnt lgkmcnt(0)\n"
"ds_read_b64_tr_b16 a[184:185], v122 offset:16384\n"
"ds_read_b64_tr_b16 a[186:187], v122 offset:16896\n"
"ds_read_b64_tr_b16 a[188:189], v122 offset:24576\n"
"ds_read_b64_tr_b16 a[190:191], v122 offset:25088\n"
"ds_read_b64_tr_b16 a[248:249], v123 offset:16384\n"
"ds_read_b64_tr_b16 a[250:251], v123 offset:16896\n"
"ds_read_b64_tr_b16 a[252:253], v123 offset:24576\n"
"ds_read_b64_tr_b16 a[254:255], v123 offset:25088\n"
"s_waitcnt lgkmcnt(0)\n"
"ds_read_b64_tr_b16 v[184:185], v124 offset:0\n"
"ds_read_b64_tr_b16 v[186:187], v124 offset:256\n"
"ds_read_b64_tr_b16 v[188:189], v124 offset:4096\n"
"ds_read_b64_tr_b16 v[190:191], v124 offset:4352\n"
"s_branch CH_PE3\n"
"CH_PW3_2:\n"
"ds_read_b64_tr_b16 a[40:41], v122 offset:0\n"
"ds_read_b64_tr_b16 a[42:43], v122 offset:512\n"
"ds_read_b64_tr_b16 a[44:45], v122 offset:8192\n"
"ds_read_b64_tr_b16 a[46:47], v122 offset:8704\n"
"ds_read_b64_tr_b16 a[104:105], v123 offset:0\n"
"ds_read_b64_tr_b16 a[106:107], v123 offset:512\n"
"ds_read_b64_tr_b16 a[108:109], v123 offset:8192\n"
"ds_read_b64_tr_b16 a[110:111], v123 offset:8704\n"
"s_waitcnt lgkmcnt(0)\n"
"ds_read_b64_tr_b16 a[168:169], v122 offset:16384\n"
"ds_read_b64_tr_b16 a[170:171], v122 offset:16896\n"
"ds_read_b64_tr_b16 a[172:173], v122 offset:24576\n"
"ds_read_b64_tr_b16 a[174:175], v122 offset:25088\n"
"ds_read_b64_tr_b16 a[232:233], v123 offset:16384\n"
"ds_read_b64_tr_b16 a[234:235], v123 offset:16896\n"
"ds_read_b64_tr_b16 a[236:237], v123 offset:24576\n"
"ds_read_b64_tr_b16 a[238:239], v123 offset:25088\n"
"s_waitcnt lgkmcnt(0)\n"
"ds_read_b64_tr_b16 v[168:169], v124 offset:0\n"
"ds_read_b64_tr_b16 v[170:171], v124 offset:256\n"
"ds_read_b64_tr_b16 v[172:173], v124 offset:4096\n"
"ds_read_b64_tr_b16 v[174:175], v124 offset:4352\n"
"CH_PE3:\n"
"s_waitcnt lgkmcnt(0)\n"
"s_barrier\n"
"s_add_u32 m0, s66, 32768\n"
"s_add_u32 s68, s56, 3145728\n"
"s_addc_u32 s69, s57, 0\n"
"global_load_lds_dwordx4 v106, s[68:69]\n"
"s_add_u32 m0, s66, 36864\n"
"s_add_u32 s68, s56, 3276800\n"
"s_addc_u32 s69, s57, 0\n"
"global_load_lds_dwordx4 v107, s[68:69]\n"
"s_add_u32 m0, s66, 40960\n"
"s_add_u32 s68, s56, 3407872\n"
"s_addc_u32 s69, s57, 0\n"
"global_load_lds_dwordx4 v106, s[68:69]\n"
"s_add_u32 m0, s66, 45056\n"
"s_add_u32 s68, s56, 3538944\n"
"s_addc_u32 s69, s57, 0\n"
"global_load_lds_dwordx4 v107, s[68:69]\n"
"s_add_u32 m0, s66, 49152\n"
"s_add_u32 s68, s58, 3145728\n"
"s_addc_u32 s69, s59, 0\n"
"global_load_lds_dwordx4 v106, s[68:69]\n"
"s_add_u32 m0, s66, 53248\n"
"s_add_u32 s68, s58, 3276800\n"
"s_addc_u32 s69, s59, 0\n"
"global_load_lds_dwordx4 v107, s[68:69]\n"
"s_add_u32 m0, s66, 57344\n"
"s_add_u32 s68, s58, 3407872\n"
"s_addc_u32 s69, s59, 0\n"
"global_load_lds_dwordx4 v106, s[68:69]\n"
"s_add_u32 m0, s66, 61440\n"
"s_add_u32 s68, s58, 3538944\n"
"s_addc_u32 s69, s59, 0\n"
"global_load_lds_dwordx4 v107, s[68:69]\n"
"s_add_u32 m0, s66, 65536\n"
"s_add_u32 s68, s60, 3145728\n"
"s_addc_u32 s69, s61, 0\n"
"global_load_lds_dwordx4 v108, s[68:69]\n"
"s_add_u32 m0, s66, 69632\n"
"s_add_u32 s68, s60, 3407872\n"
"s_addc_u32 s69, s61, 0\n"
"global_load_lds_dwordx4 v108, s[68:69]\n"
"s_waitcnt vmcnt(20)\n"
"s_barrier\n"
"v_add_u32 v122, 73728, v112\n"
"v_add_u32 v123, 73728, v113\n"
"v_add_u32 v124, 106496, v114\n"
"s_cmp_eq_u32 s71, 0\n"
"s_cbranch_scc0 CH_PW4_0\n"
"ds_read_b64_tr_b16 a[32:33], v122 offset:0\n"
"ds_read_b64_tr_b16 a[34:35], v122 offset:512\n"
"ds_read_b64_tr_b16 a[36:37], v122 offset:8192\n"
"ds_read_b64_tr_b16 a[38:39], v122 offset:8704\n"
"ds_read_b64_tr_b16 a[96:97], v123 offset:0\n"
"ds_read_b64_tr_b16 a[98:99], v123 offset:512\n"
"ds_read_b64_tr_b16 a[100:101], v123 offset:8192\n"
"ds_read_b64_tr_b16 a[102:103], v123 offset:8704\n"
"s_waitcnt lgkmcnt(0)\n"
"ds_read_b64_tr_b16 a[160:161], v122 offset:16384\n"
"ds_read_b64_tr_b16 a[162:163], v122 offset:16896\n"
"ds_read_b64_tr_b16 a[164:165], v122 offset:24576\n"
"ds_read_b64_tr_b16 a[166:167], v122 offset:25088\n"
"ds_read_b64_tr_b16 a[224:225], v123 offset:16384\n"
"ds_read_b64_tr_b16 a[226:227], v123 offset:16896\n"
"ds_read_b64_tr_b16 a[228:229], v123 offset:24576\n"
"ds_read_b64_tr_b16 a[230:231], v123 offset:25088\n"
"s_waitcnt lgkmcnt(0)\n"
"ds_read_b64_tr_b16 v[160:161], v124 offset:0\n"
"ds_read_b64_tr_b16 v[162:163], v124 offset:256\n"
"ds_read_b64_tr_b16 v[164:165], v124 offset:4096\n"
"ds_read_b64_tr_b16 v[166:167], v124 offset:4352\n"
"s_branch CH_PE4\n"
"CH_PW4_0:\n"
"s_cmp_eq_u32 s71, 1\n"
"s_cbranch_scc0 CH_PW4_1\n"
"ds_read_b64_tr_b16 a[48:49], v122 offset:0\n"
"ds_read_b64_tr_b16 a[50:51], v122 offset:512\n"
"ds_read_b64_tr_b16 a[52:53], v122 offset:8192\n"
"ds_read_b64_tr_b16 a[54:55], v122 offset:8704\n"
"ds_read_b64_tr_b16 a[112:113], v123 offset:0\n"
"ds_read_b64_tr_b16 a[114:115], v123 offset:512\n"
"ds_read_b64_tr_b16 a[116:117], v123 offset:8192\n"
"ds_read_b64_tr_b16 a[118:119], v123 offset:8704\n"
"s_waitcnt lgkmcnt(0)\n"
"ds_read_b64_tr_b16 a[176:177], v122 offset:16384\n"
"ds_read_b64_tr_b16 a[178:179], v122 offset:16896\n"
"ds_read_b64_tr_b16 a[180:181], v122 offset:24576\n"
"ds_read_b64_tr_b16 a[182:183], v122 offset:25088\n"
"ds_read_b64_tr_b16 a[240:241], v123 offset:16384\n"
"ds_read_b64_tr_b16 a[242:243], v123 offset:16896\n"
"ds_read_b64_tr_b16 a[244:245], v123 offset:24576\n"
"ds_read_b64_tr_b16 a[246:247], v123 offset:25088\n"
"s_waitcnt lgkmcnt(0)\n"
"ds_read_b64_tr_b16 v[176:177], v124 offset:0\n"
"ds_read_b64_tr_b16 v[178:179], v124 offset:256\n"
"ds_read_b64_tr_b16 v[180:181], v124 offset:4096\n"
"ds_read_b64_tr_b16 v[182:183], v124 offset:4352\n"
"s_branch CH_PE4\n"
"CH_PW4_1:\n"
"s_cmp_eq_u32 s71, 2\n"
"s_cbranch_scc0 CH_PW4_2\n"
"ds_read_b64_tr_b16 a[0:1], v122 offset:0\n"
"ds_read_b64_tr_b16 a[2:3], v122 offset:512\n"
"ds_read_b64_tr_b16 a[4:5], v122 offset:8192\n"
"ds_read_b64_tr_b16 a[6:7], v122 offset:8704\n"
"ds_read_b64_tr_b16 a[64:65], v123 offset:0\n"
"ds_read_b64_tr_b16 a[66:67], v123 offset:512\n"
"ds_read_b64_tr_b16 a[68:69], v123 offset:8192\n"
"ds_read_b64_tr_b16 a[70:71], v123 offset:8704\n"
"s_waitcnt lgkmcnt(0)\n"
"ds_read_b64_tr_b16 a[128:129], v122 offset:16384\n"
"ds_read_b64_tr_b16 a[130:131], v122 offset:16896\n"
"ds_read_b64_tr_b16 a[132:133], v122 offset:24576\n"
"ds_read_b64_tr_b16 a[134:135], v122 offset:25088\n"
"ds_read_b64_tr_b16 a[192:193], v123 offset:16384\n"
"ds_read_b64_tr_b16 a[194:195], v123 offset:16896\n"
"ds_read_b64_tr_b16 a[196:197], v123 offset:24576\n"
"ds_read_b64_tr_b16 a[198:199], v123 offset:25088\n"
"s_waitcnt lgkmcnt(0)\n"
"ds_read_b64_tr_b16 v[128:129], v124 offset:0\n"
"ds_read_b64_tr_b16 v[130:131], v124 offset:256\n"
"ds_read_b64_tr_b16 v[132:133], v124 offset:4096\n"
"ds_read_b64_tr_b16 v[134:135], v124 offset:4352\n"
"s_branch CH_PE4\n"
"CH_PW4_2:\n"
"ds_read_b64_tr_b16 a[16:17], v122 offset:0\n"
"ds_read_b64_tr_b16 a[18:19], v122 offset:512\n"
"ds_read_b64_tr_b16 a[20:21], v122 offset:8192\n"
"ds_read_b64_tr_b16 a[22:23], v122 offset:8704\n"
"ds_read_b64_tr_b16 a[80:81], v123 offset:0\n"
"ds_read_b64_tr_b16 a[82:83], v123 offset:512\n"
"ds_read_b64_tr_b16 a[84:85], v123 offset:8192\n"
"ds_read_b64_tr_b16 a[86:87], v123 offset:8704\n"
"s_waitcnt lgkmcnt(0)\n"
"ds_read_b64_tr_b16 a[144:145], v122 offset:16384\n"
"ds_read_b64_tr_b16 a[146:147], v122 offset:16896\n"
"ds_read_b64_tr_b16 a[148:149], v122 offset:24576\n"
"ds_read_b64_tr_b16 a[150:151], v122 offset:25088\n"
"ds_read_b64_tr_b16 a[208:209], v123 offset:16384\n"
"ds_read_b64_tr_b16 a[210:211], v123 offset:16896\n"
"ds_read_b64_tr_b16 a[212:213], v123 offset:24576\n"
"ds_read_b64_tr_b16 a[214:215], v123 offset:25088\n"
"s_waitcnt lgkmcnt(0)\n"
"ds_read_b64_tr_b16 v[144:145], v124 offset:0\n"
"ds_read_b64_tr_b16 v[146:147], v124 offset:256\n"
"ds_read_b64_tr_b16 v[148:149], v124 offset:4096\n"
"ds_read_b64_tr_b16 v[150:151], v124 offset:4352\n"
"CH_PE4:\n"
"s_waitcnt lgkmcnt(0)\n"
"s_barrier\n"
"s_add_u32 m0, s66, 73728\n"
"s_add_u32 s68, s56, 3670016\n"
"s_addc_u32 s69, s57, 0\n"
"global_load_lds_dwordx4 v106, s[68:69]\n"
"s_add_u32 m0, s66, 77824\n"
"s_add_u32 s68, s56, 3801088\n"
"s_addc_u32 s69, s57, 0\n"
"global_load_lds_dwordx4 v107, s[68:69]\n"
"s_add_u32 m0, s66, 81920\n"
"s_add_u32 s68, s56, 3932160\n"
"s_addc_u32 s69, s57, 0\n"
"global_load_lds_dwordx4 v106, s[68:69]\n"
"s_add_u32 m0, s66, 86016\n"
"s_add_u32 s68, s56, 4063232\n"
"s_addc_u32 s69, s57, 0\n"
"global_load_lds_dwordx4 v107, s[68:69]\n"
"s_add_u32 m0, s66, 90112\n"
"s_add_u32 s68, s58, 3670016\n"
"s_addc_u32 s69, s59, 0\n"
"global_load_lds_dwordx4 v106, s[68:69]\n"
"s_add_u32 m0, s66, 94208\n"
"s_add_u32 s68, s58, 3801088\n"
"s_addc_u32 s69, s59, 0\n"
"global_load_lds_dwordx4 v107, s[68:69]\n"
"s_add_u32 m0, s66, 98304\n"
"s_add_u32 s68, s58, 3932160\n"
"s_addc_u32 s69, s59, 0\n"
"global_load_lds_dwordx4 v106, s[68:69]\n"
"s_add_u32 m0, s66, 102400\n"
"s_add_u32 s68, s58, 4063232\n"
"s_addc_u32 s69, s59, 0\n"
"global_load_lds_dwordx4 v107, s[68:69]\n"
"s_add_u32 m0, s66, 106496\n"
"s_add_u32 s68, s60, 3670016\n"
"s_addc_u32 s69, s61, 0\n"
"global_load_lds_dwordx4 v108, s[68:69]\n"
"s_add_u32 m0, s66, 110592\n"
"s_add_u32 s68, s60, 3932160\n"
"s_addc_u32 s69, s61, 0\n"
"global_load_lds_dwordx4 v108, s[68:69]\n"
"s_waitcnt vmcnt(20)\n"
"s_barrier\n"
"v_add_u32 v122, 114688, v112\n"
"v_add_u32 v123, 114688, v113\n"
"v_add_u32 v124, 147456, v114\n"
"s_cmp_eq_u32 s71, 0\n"
"s_cbranch_scc0 CH_PW5_0\n"
"ds_read_b64_tr_b16 a[40:41], v122 offset:0\n"
"ds_read_b64_tr_b16 a[42:43], v122 offset:512\n"
"ds_read_b64_tr_b16 a[44:45], v122 offset:8192\n"
"ds_read_b64_tr_b16 a[46:47], v122 offset:8704\n"
"ds_read_b64_tr_b16 a[104:105], v123 offset:0\n"
"ds_read_b64_tr_b16 a[106:107], v123 offset:512\n"
"ds_read_b64_tr_b16 a[108:109], v123 offset:8192\n"
"ds_read_b64_tr_b16 a[110:111], v123 offset:8704\n"
"s_waitcnt lgkmcnt(0)\n"
"ds_read_b64_tr_b16 a[168:169], v122 offset:16384\n"
"ds_read_b64_tr_b16 a[170:171], v122 offset:16896\n"
"ds_read_b64_tr_b16 a[172:173], v122 offset:24576\n"
"ds_read_b64_tr_b16 a[174:175], v122 offset:25088\n"
"ds_read_b64_tr_b16 a[232:233], v123 offset:16384\n"
"ds_read_b64_tr_b16 a[234:235], v123 offset:16896\n"
"ds_read_b64_tr_b16 a[236:237], v123 offset:24576\n"
"ds_read_b64_tr_b16 a[238:239], v123 offset:25088\n"
"s_waitcnt lgkmcnt(0)\n"
"ds_read_b64_tr_b16 v[168:169], v124 offset:0\n"
"ds_read_b64_tr_b16 v[170:171], v124 offset:256\n"
"ds_read_b64_tr_b16 v[172:173], v124 offset:4096\n"
"ds_read_b64_tr_b16 v[174:175], v124 offset:4352\n"
"s_branch CH_PE5\n"
"CH_PW5_0:\n"
"s_cmp_eq_u32 s71, 1\n"
"s_cbranch_scc0 CH_PW5_1\n"
"ds_read_b64_tr_b16 a[56:57], v122 offset:0\n"
"ds_read_b64_tr_b16 a[58:59], v122 offset:512\n"
"ds_read_b64_tr_b16 a[60:61], v122 offset:8192\n"
"ds_read_b64_tr_b16 a[62:63], v122 offset:8704\n"
"ds_read_b64_tr_b16 a[120:121], v123 offset:0\n"
"ds_read_b64_tr_b16 a[122:123], v123 offset:512\n"
"ds_read_b64_tr_b16 a[124:125], v123 offset:8192\n"
"ds_read_b64_tr_b16 a[126:127], v123 offset:8704\n"
"s_waitcnt lgkmcnt(0)\n"
"ds_read_b64_tr_b16 a[184:185], v122 offset:16384\n"
"ds_read_b64_tr_b16 a[186:187], v122 offset:16896\n"
"ds_read_b64_tr_b16 a[188:189], v122 offset:24576\n"
"ds_read_b64_tr_b16 a[190:191], v122 offset:25088\n"
"ds_read_b64_tr_b16 a[248:249], v123 offset:16384\n"
"ds_read_b64_tr_b16 a[250:251], v123 offset:16896\n"
"ds_read_b64_tr_b16 a[252:253], v123 offset:24576\n"
"ds_read_b64_tr_b16 a[254:255], v123 offset:25088\n"
"s_waitcnt lgkmcnt(0)\n"
"ds_read_b64_tr_b16 v[184:185], v124 offset:0\n"
"ds_read_b64_tr_b16 v[186:187], v124 offset:256\n"
"ds_read_b64_tr_b16 v[188:189], v124 offset:4096\n"
"ds_read_b64_tr_b16 v[190:191], v124 offset:4352\n"
"s_branch CH_PE5\n"
"CH_PW5_1:\n"
"s_cmp_eq_u32 s71, 2\n"
"s_cbranch_scc0 CH_PW5_2\n"
"ds_read_b64_tr_b16 a[8:9], v122 offset:0\n"
"ds_read_b64_tr_b16 a[10:11], v122 offset:512\n"
"ds_read_b64_tr_b16 a[12:13], v122 offset:8192\n"
"ds_read_b64_tr_b16 a[14:15], v122 offset:8704\n"
"ds_read_b64_tr_b16 a[72:73], v123 offset:0\n"
"ds_read_b64_tr_b16 a[74:75], v123 offset:512\n"
"ds_read_b64_tr_b16 a[76:77], v123 offset:8192\n"
"ds_read_b64_tr_b16 a[78:79], v123 offset:8704\n"
"s_waitcnt lgkmcnt(0)\n"
"ds_read_b64_tr_b16 a[136:137], v122 offset:16384\n"
"ds_read_b64_tr_b16 a[138:139], v122 offset:16896\n"
"ds_read_b64_tr_b16 a[140:141], v122 offset:24576\n"
"ds_read_b64_tr_b16 a[142:143], v122 offset:25088\n"
"ds_read_b64_tr_b16 a[200:201], v123 offset:16384\n"
"ds_read_b64_tr_b16 a[202:203], v123 offset:16896\n"
"ds_read_b64_tr_b16 a[204:205], v123 offset:24576\n"
"ds_read_b64_tr_b16 a[206:207], v123 offset:25088\n"
"s_waitcnt lgkmcnt(0)\n"
"ds_read_b64_tr_b16 v[136:137], v124 offset:0\n"
"ds_read_b64_tr_b16 v[138:139], v124 offset:256\n"
"ds_read_b64_tr_b16 v[140:141], v124 offset:4096\n"
"ds_read_b64_tr_b16 v[142:143], v124 offset:4352\n"
"s_branch CH_PE5\n"
"CH_PW5_2:\n"
"ds_read_b64_tr_b16 a[24:25], v122 offset:0\n"
"ds_read_b64_tr_b16 a[26:27], v122 offset:512\n"
"ds_read_b64_tr_b16 a[28:29], v122 offset:8192\n"
"ds_read_b64_tr_b16 a[30:31], v122 offset:8704\n"
"ds_read_b64_tr_b16 a[88:89], v123 offset:0\n"
"ds_read_b64_tr_b16 a[90:91], v123 offset:512\n"
"ds_read_b64_tr_b16 a[92:93], v123 offset:8192\n"
"ds_read_b64_tr_b16 a[94:95], v123 offset:8704\n"
"s_waitcnt lgkmcnt(0)\n"
"ds_read_b64_tr_b16 a[152:153], v122 offset:16384\n"
"ds_read_b64_tr_b16 a[154:155], v122 offset:16896\n"
"ds_read_b64_tr_b16 a[156:157], v122 offset:24576\n"
"ds_read_b64_tr_b16 a[158:159], v122 offset:25088\n"
"ds_read_b64_tr_b16 a[216:217], v123 offset:16384\n"
"ds_read_b64_tr_b16 a[218:219], v123 offset:16896\n"
"ds_read_b64_tr_b16 a[220:221], v123 offset:24576\n"
"ds_read_b64_tr_b16 a[222:223], v123 offset:25088\n"
"s_waitcnt lgkmcnt(0)\n"
"ds_read_b64_tr_b16 v[152:153], v124 offset:0\n"
"ds_read_b64_tr_b16 v[154:155], v124 offset:256\n"
"ds_read_b64_tr_b16 v[156:157], v124 offset:4096\n"
"ds_read_b64_tr_b16 v[158:159], v124 offset:4352\n"
"CH_PE5:\n"
"s_waitcnt lgkmcnt(0)\n"
"s_barrier\n"
"s_lshl_b32 s44, s41, 13\n"
"s_add_u32 m0, s44, 114688\n"
"v_xor_b32 v110, 0, v109\n"
"s_add_u32 s68, s62, 0\n"
"s_addc_u32 s69, s63, 0\n"
"global_load_lds_dwordx4 v110, s[68:69]\n"
"s_lshl_b32 s44, s41, 13\n"
"s_add_u32 m0, s44, 115712\n"
"v_xor_b32 v110, 32, v109\n"
"s_add_u32 s68, s62, 1024\n"
"s_addc_u32 s69, s63, 0\n"
"global_load_lds_dwordx4 v110, s[68:69]\n"
"s_lshl_b32 s44, s41, 13\n"
"s_add_u32 m0, s44, 116736\n"
"v_xor_b32 v110, 0, v109\n"
"s_add_u32 s68, s62, 2048\n"
"s_addc_u32 s69, s63, 0\n"
"global_load_lds_dwordx4 v110, s[68:69]\n"
"s_lshl_b32 s44, s41, 13\n"
"s_add_u32 m0, s44, 117760\n"
"v_xor_b32 v110, 32, v109\n"
"s_add_u32 s68, s62, 3072\n"
"s_addc_u32 s69, s63, 0\n"
"global_load_lds_dwordx4 v110, s[68:69]\n"
"s_lshl_b32 s44, s41, 13\n"
"s_add_u32 m0, s44, 118784\n"
"v_xor_b32 v110, 64, v109\n"
"s_add_u32 s68, s62, 4096\n"
"s_addc_u32 s69, s63, 0\n"
"global_load_lds_dwordx4 v110, s[68:69]\n"
"s_lshl_b32 s44, s41, 13\n"
"s_add_u32 m0, s44, 119808\n"
"v_xor_b32 v110, 96, v109\n"
"s_add_u32 s68, s62, 5120\n"
"s_addc_u32 s69, s63, 0\n"
"global_load_lds_dwordx4 v110, s[68:69]\n"
"s_lshl_b32 s44, s41, 13\n"
"s_add_u32 m0, s44, 120832\n"
"v_xor_b32 v110, 64, v109\n"
"s_add_u32 s68, s62, 6144\n"
"s_addc_u32 s69, s63, 0\n"
"global_load_lds_dwordx4 v110, s[68:69]\n"
"s_lshl_b32 s44, s41, 13\n"
"s_add_u32 m0, s44, 121856\n"
"v_xor_b32 v110, 96, v109\n"
"s_add_u32 s68, s62, 7168\n"
"s_addc_u32 s69, s63, 0\n"
"global_load_lds_dwordx4 v110, s[68:69]\n"
"s_waitcnt vmcnt(18)\n"
"s_barrier\n"
"v_add_u32 v122, 32768, v112\n"
"v_add_u32 v123, 32768, v113\n"
"v_add_u32 v124, 65536, v114\n"
"s_cmp_eq_u32 s71, 0\n"
"s_cbranch_scc0 CH_PW6_0\n"
"ds_read_b64_tr_b16 a[48:49], v122 offset:0\n"
"ds_read_b64_tr_b16 a[50:51], v122 offset:512\n"
"ds_read_b64_tr_b16 a[52:53], v122 offset:8192\n"
"ds_read_b64_tr_b16 a[54:55], v122 offset:8704\n"
"ds_read_b64_tr_b16 a[112:113], v123 offset:0\n"
"ds_read_b64_tr_b16 a[114:115], v123 offset:512\n"
"ds_read_b64_tr_b16 a[116:117], v123 offset:8192\n"
"ds_read_b64_tr_b16 a[118:119], v123 offset:8704\n"
"s_waitcnt lgkmcnt(0)\n"
"ds_read_b64_tr_b16 a[176:177], v122 offset:16384\n"
"ds_read_b64_tr_b16 a[178:179], v122 offset:16896\n"
"ds_read_b64_tr_b16 a[180:181], v122 offset:24576\n"
"ds_read_b64_tr_b16 a[182:183], v122 offset:25088\n"
"ds_read_b64_tr_b16 a[240:241], v123 offset:16384\n"
"ds_read_b64_tr_b16 a[242:243], v123 offset:16896\n"
"ds_read_b64_tr_b16 a[244:245], v123 offset:24576\n"
"ds_read_b64_tr_b16 a[246:247], v123 offset:25088\n"
"s_waitcnt lgkmcnt(0)\n"
"ds_read_b64_tr_b16 v[176:177], v124 offset:0\n"
"ds_read_b64_tr_b16 v[178:179], v124 offset:256\n"
"ds_read_b64_tr_b16 v[180:181], v124 offset:4096\n"
"ds_read_b64_tr_b16 v[182:183], v124 offset:4352\n"
"s_branch CH_PE6\n"
"CH_PW6_0:\n"
"s_cmp_eq_u32 s71, 1\n"
"s_cbranch_scc0 CH_PW6_1\n"
"ds_read_b64_tr_b16 a[32:33], v122 offset:0\n"
"ds_read_b64_tr_b16 a[34:35], v122 offset:512\n"
"ds_read_b64_tr_b16 a[36:37], v122 offset:8192\n"
"ds_read_b64_tr_b16 a[38:39], v122 offset:8704\n"
"ds_read_b64_tr_b16 a[96:97], v123 offset:0\n"
"ds_read_b64_tr_b16 a[98:99], v123 offset:512\n"
"ds_read_b64_tr_b16 a[100:101], v123 offset:8192\n"
"ds_read_b64_tr_b16 a[102:103], v123 offset:8704\n"
"s_waitcnt lgkmcnt(0)\n"
"ds_read_b64_tr_b16 a[160:161], v122 offset:16384\n"
"ds_read_b64_tr_b16 a[162:163], v122 offset:16896\n"
"ds_read_b64_tr_b16 a[164:165], v122 offset:24576\n"
"ds_read_b64_tr_b16 a[166:167], v122 offset:25088\n"
"ds_read_b64_tr_b16 a[224:225], v123 offset:16384\n"
"ds_read_b64_tr_b16 a[226:227], v123 offset:16896\n"
"ds_read_b64_tr_b16 a[228:229], v123 offset:24576\n"
"ds_read_b64_tr_b16 a[230:231], v123 offset:25088\n"
"s_waitcnt lgkmcnt(0)\n"
"ds_read_b64_tr_b16 v[160:161], v124 offset:0\n"
"ds_read_b64_tr_b16 v[162:163], v124 offset:256\n"
"ds_read_b64_tr_b16 v[164:165], v124 offset:4096\n"
"ds_read_b64_tr_b16 v[166:167], v124 offset:4352\n"
"s_branch CH_PE6\n"
"CH_PW6_1:\n"
"s_cmp_eq_u32 s71, 2\n"
"s_cbranch_scc0 CH_PW6_2\n"
"ds_read_b64_tr_b16 a[16:17], v122 offset:0\n"
"ds_read_b64_tr_b16 a[18:19], v122 offset:512\n"
"ds_read_b64_tr_b16 a[20:21], v122 offset:8192\n"
"ds_read_b64_tr_b16 a[22:23], v122 offset:8704\n"
"ds_read_b64_tr_b16 a[80:81], v123 offset:0\n"
"ds_read_b64_tr_b16 a[82:83], v123 offset:512\n"
"ds_read_b64_tr_b16 a[84:85], v123 offset:8192\n"
"ds_read_b64_tr_b16 a[86:87], v123 offset:8704\n"
"s_waitcnt lgkmcnt(0)\n"
"ds_read_b64_tr_b16 a[144:145], v122 offset:16384\n"
"ds_read_b64_tr_b16 a[146:147], v122 offset:16896\n"
"ds_read_b64_tr_b16 a[148:149], v122 offset:24576\n"
"ds_read_b64_tr_b16 a[150:151], v122 offset:25088\n"
"ds_read_b64_tr_b16 a[208:209], v123 offset:16384\n"
"ds_read_b64_tr_b16 a[210:211], v123 offset:16896\n"
"ds_read_b64_tr_b16 a[212:213], v123 offset:24576\n"
"ds_read_b64_tr_b16 a[214:215], v123 offset:25088\n"
"s_waitcnt lgkmcnt(0)\n"
"ds_read_b64_tr_b16 v[144:145], v124 offset:0\n"
"ds_read_b64_tr_b16 v[146:147], v124 offset:256\n"
"ds_read_b64_tr_b16 v[148:149], v124 offset:4096\n"
"ds_read_b64_tr_b16 v[150:151], v124 offset:4352\n"
"s_branch CH_PE6\n"
"CH_PW6_2:\n"
"ds_read_b64_tr_b16 a[0:1], v122 offset:0\n"
"ds_read_b64_tr_b16 a[2:3], v122 offset:512\n"
"ds_read_b64_tr_b16 a[4:5], v122 offset:8192\n"
"ds_read_b64_tr_b16 a[6:7], v122 offset:8704\n"
"ds_read_b64_tr_b16 a[64:65], v123 offset:0\n"
"ds_read_b64_tr_b16 a[66:67], v123 offset:512\n"
"ds_read_b64_tr_b16 a[68:69], v123 offset:8192\n"
"ds_read_b64_tr_b16 a[70:71], v123 offset:8704\n"
"s_waitcnt lgkmcnt(0)\n"
"ds_read_b64_tr_b16 a[128:129], v122 offset:16384\n"
"ds_read_b64_tr_b16 a[130:131], v122 offset:16896\n"
"ds_read_b64_tr_b16 a[132:133], v122 offset:24576\n"
"ds_read_b64_tr_b16 a[134:135], v122 offset:25088\n"
"ds_read_b64_tr_b16 a[192:193], v123 offset:16384\n"
"ds_read_b64_tr_b16 a[194:195], v123 offset:16896\n"
"ds_read_b64_tr_b16 a[196:197], v123 offset:24576\n"
"ds_read_b64_tr_b16 a[198:199], v123 offset:25088\n"
"s_waitcnt lgkmcnt(0)\n"
"ds_read_b64_tr_b16 v[128:129], v124 offset:0\n"
"ds_read_b64_tr_b16 v[130:131], v124 offset:256\n"
"ds_read_b64_tr_b16 v[132:133], v124 offset:4096\n"
"ds_read_b64_tr_b16 v[134:135], v124 offset:4352\n"
"CH_PE6:\n"
"s_waitcnt lgkmcnt(0)\n"
"s_barrier\n"
"s_lshl_b32 s44, s41, 13\n"
"s_add_u32 m0, s44, 32768\n"
"v_xor_b32 v110, 0, v109\n"
"s_add_u32 s68, s62, 16384\n"
"s_addc_u32 s69, s63, 0\n"
"global_load_lds_dwordx4 v110, s[68:69]\n"
"s_lshl_b32 s44, s41, 13\n"
"s_add_u32 m0, s44, 33792\n"
"v_xor_b32 v110, 32, v109\n"
"s_add_u32 s68, s62, 17408\n"
"s_addc_u32 s69, s63, 0\n"
"global_load_lds_dwordx4 v110, s[68:69]\n"
"s_lshl_b32 s44, s41, 13\n"
"s_add_u32 m0, s44, 34816\n"
"v_xor_b32 v110, 0, v109\n"
"s_add_u32 s68, s62, 18432\n"
"s_addc_u32 s69, s63, 0\n"
"global_load_lds_dwordx4 v110, s[68:69]\n"
"s_lshl_b32 s44, s41, 13\n"
"s_add_u32 m0, s44, 35840\n"
"v_xor_b32 v110, 32, v109\n"
"s_add_u32 s68, s62, 19456\n"
"s_addc_u32 s69, s63, 0\n"
"global_load_lds_dwordx4 v110, s[68:69]\n"
"s_lshl_b32 s44, s41, 13\n"
"s_add_u32 m0, s44, 36864\n"
"v_xor_b32 v110, 64, v109\n"
"s_add_u32 s68, s62, 20480\n"
"s_addc_u32 s69, s63, 0\n"
"global_load_lds_dwordx4 v110, s[68:69]\n"
"s_lshl_b32 s44, s41, 13\n"
"s_add_u32 m0, s44, 37888\n"
"v_xor_b32 v110, 96, v109\n"
"s_add_u32 s68, s62, 21504\n"
"s_addc_u32 s69, s63, 0\n"
"global_load_lds_dwordx4 v110, s[68:69]\n"
"s_lshl_b32 s44, s41, 13\n"
"s_add_u32 m0, s44, 38912\n"
"v_xor_b32 v110, 64, v109\n"
"s_add_u32 s68, s62, 22528\n"
"s_addc_u32 s69, s63, 0\n"
"global_load_lds_dwordx4 v110, s[68:69]\n"
"s_lshl_b32 s44, s41, 13\n"
"s_add_u32 m0, s44, 39936\n"
"v_xor_b32 v110, 96, v109\n"
"s_add_u32 s68, s62, 23552\n"
"s_addc_u32 s69, s63, 0\n"
"global_load_lds_dwordx4 v110, s[68:69]\n"
"s_waitcnt vmcnt(16)\n"
"s_barrier\n"
"v_add_u32 v122, 73728, v112\n"
"v_add_u32 v123, 73728, v113\n"
"v_add_u32 v124, 106496, v114\n"
"s_cmp_eq_u32 s71, 0\n"
"s_cbranch_scc0 CH_PW7_0\n"
"ds_read_b64_tr_b16 a[56:57], v122 offset:0\n"
"ds_read_b64_tr_b16 a[58:59], v122 offset:512\n"
"ds_read_b64_tr_b16 a[60:61], v122 offset:8192\n"
"ds_read_b64_tr_b16 a[62:63], v122 offset:8704\n"
"ds_read_b64_tr_b16 a[120:121], v123 offset:0\n"
"ds_read_b64_tr_b16 a[122:123], v123 offset:512\n"
"ds_read_b64_tr_b16 a[124:125], v123 offset:8192\n"
"ds_read_b64_tr_b16 a[126:127], v123 offset:8704\n"
"s_waitcnt lgkmcnt(0)\n"
"ds_read_b64_tr_b16 a[184:185], v122 offset:16384\n"
"ds_read_b64_tr_b16 a[186:187], v122 offset:16896\n"
"ds_read_b64_tr_b16 a[188:189], v122 offset:24576\n"
"ds_read_b64_tr_b16 a[190:191], v122 offset:25088\n"
"ds_read_b64_tr_b16 a[248:249], v123 offset:16384\n"
"ds_read_b64_tr_b16 a[250:251], v123 offset:16896\n"
"ds_read_b64_tr_b16 a[252:253], v123 offset:24576\n"
"ds_read_b64_tr_b16 a[254:255], v123 offset:25088\n"
"s_waitcnt lgkmcnt(0)\n"
"ds_read_b64_tr_b16 v[184:185], v124 offset:0\n"
"ds_read_b64_tr_b16 v[186:187], v124 offset:256\n"
"ds_read_b64_tr_b16 v[188:189], v124 offset:4096\n"
"ds_read_b64_tr_b16 v[190:191], v124 offset:4352\n"
"s_branch CH_PE7\n"
"CH_PW7_0:\n"
"s_cmp_eq_u32 s71, 1\n"
"s_cbranch_scc0 CH_PW7_1\n"
"ds_read_b64_tr_b16 a[40:41], v122 offset:0\n"
"ds_read_b64_tr_b16 a[42:43], v122 offset:512\n"
"ds_read_b64_tr_b16 a[44:45], v122 offset:8192\n"
"ds_read_b64_tr_b16 a[46:47], v122 offset:8704\n"
"ds_read_b64_tr_b16 a[104:105], v123 offset:0\n"
"ds_read_b64_tr_b16 a[106:107], v123 offset:512\n"
"ds_read_b64_tr_b16 a[108:109], v123 offset:8192\n"
"ds_read_b64_tr_b16 a[110:111], v123 offset:8704\n"
"s_waitcnt lgkmcnt(0)\n"
"ds_read_b64_tr_b16 a[168:169], v122 offset:16384\n"
"ds_read_b64_tr_b16 a[170:171], v122 offset:16896\n"
"ds_read_b64_tr_b16 a[172:173], v122 offset:24576\n"
"ds_read_b64_tr_b16 a[174:175], v122 offset:25088\n"
"ds_read_b64_tr_b16 a[232:233], v123 offset:16384\n"
"ds_read_b64_tr_b16 a[234:235], v123 offset:16896\n"
"ds_read_b64_tr_b16 a[236:237], v123 offset:24576\n"
"ds_read_b64_tr_b16 a[238:239], v123 offset:25088\n"
"s_waitcnt lgkmcnt(0)\n"
"ds_read_b64_tr_b16 v[168:169], v124 offset:0\n"
"ds_read_b64_tr_b16 v[170:171], v124 offset:256\n"
"ds_read_b64_tr_b16 v[172:173], v124 offset:4096\n"
"ds_read_b64_tr_b16 v[174:175], v124 offset:4352\n"
"s_branch CH_PE7\n"
"CH_PW7_1:\n"
"s_cmp_eq_u32 s71, 2\n"
"s_cbranch_scc0 CH_PW7_2\n"
"ds_read_b64_tr_b16 a[24:25], v122 offset:0\n"
"ds_read_b64_tr_b16 a[26:27], v122 offset:512\n"
"ds_read_b64_tr_b16 a[28:29], v122 offset:8192\n"
"ds_read_b64_tr_b16 a[30:31], v122 offset:8704\n"
"ds_read_b64_tr_b16 a[88:89], v123 offset:0\n"
"ds_read_b64_tr_b16 a[90:91], v123 offset:512\n"
"ds_read_b64_tr_b16 a[92:93], v123 offset:8192\n"
"ds_read_b64_tr_b16 a[94:95], v123 offset:8704\n"
"s_waitcnt lgkmcnt(0)\n"
"ds_read_b64_tr_b16 a[152:153], v122 offset:16384\n"
"ds_read_b64_tr_b16 a[154:155], v122 offset:16896\n"
"ds_read_b64_tr_b16 a[156:157], v122 offset:24576\n"
"ds_read_b64_tr_b16 a[158:159], v122 offset:25088\n"
"ds_read_b64_tr_b16 a[216:217], v123 offset:16384\n"
"ds_read_b64_tr_b16 a[218:219], v123 offset:16896\n"
"ds_read_b64_tr_b16 a[220:221], v123 offset:24576\n"
"ds_read_b64_tr_b16 a[222:223], v123 offset:25088\n"
"s_waitcnt lgkmcnt(0)\n"
"ds_read_b64_tr_b16 v[152:153], v124 offset:0\n"
"ds_read_b64_tr_b16 v[154:155], v124 offset:256\n"
"ds_read_b64_tr_b16 v[156:157], v124 offset:4096\n"
"ds_read_b64_tr_b16 v[158:159], v124 offset:4352\n"
"s_branch CH_PE7\n"
"CH_PW7_2:\n"
"ds_read_b64_tr_b16 a[8:9], v122 offset:0\n"
"ds_read_b64_tr_b16 a[10:11], v122 offset:512\n"
"ds_read_b64_tr_b16 a[12:13], v122 offset:8192\n"
"ds_read_b64_tr_b16 a[14:15], v122 offset:8704\n"
"ds_read_b64_tr_b16 a[72:73], v123 offset:0\n"
"ds_read_b64_tr_b16 a[74:75], v123 offset:512\n"
"ds_read_b64_tr_b16 a[76:77], v123 offset:8192\n"
"ds_read_b64_tr_b16 a[78:79], v123 offset:8704\n"
"s_waitcnt lgkmcnt(0)\n"
"ds_read_b64_tr_b16 a[136:137], v122 offset:16384\n"
"ds_read_b64_tr_b16 a[138:139], v122 offset:16896\n"
"ds_read_b64_tr_b16 a[140:141], v122 offset:24576\n"
"ds_read_b64_tr_b16 a[142:143], v122 offset:25088\n"
"ds_read_b64_tr_b16 a[200:201], v123 offset:16384\n"
"ds_read_b64_tr_b16 a[202:203], v123 offset:16896\n"
"ds_read_b64_tr_b16 a[204:205], v123 offset:24576\n"
"ds_read_b64_tr_b16 a[206:207], v123 offset:25088\n"
"s_waitcnt lgkmcnt(0)\n"
"ds_read_b64_tr_b16 v[136:137], v124 offset:0\n"
"ds_read_b64_tr_b16 v[138:139], v124 offset:256\n"
"ds_read_b64_tr_b16 v[140:141], v124 offset:4096\n"
"ds_read_b64_tr_b16 v[142:143], v124 offset:4352\n"
"CH_PE7:\n"
"s_waitcnt lgkmcnt(0)\n"
"s_waitcnt vmcnt(8)\n"
"s_barrier\n"
"v_xor_b32 v122, 0, v115\n"
"v_add_u32 v122, 114688, v122\n"
"ds_read_b64_tr_b16 v[192:193], v122 offset:0\n"
"ds_read_b64_tr_b16 v[194:195], v122 offset:2048\n"
"v_xor_b32 v122, 32, v115\n"
"v_add_u32 v122, 114688, v122\n"
"ds_read_b64_tr_b16 v[200:201], v122 offset:0\n"
"ds_read_b64_tr_b16 v[202:203], v122 offset:2048\n"
"v_xor_b32 v122, 64, v115\n"
"v_add_u32 v122, 114688, v122\n"
"ds_read_b64_tr_b16 v[208:209], v122 offset:0\n"
"ds_read_b64_tr_b16 v[210:211], v122 offset:2048\n"
"v_xor_b32 v122, 96, v115\n"
"v_add_u32 v122, 114688, v122\n"
"ds_read_b64_tr_b16 v[216:217], v122 offset:0\n"
"ds_read_b64_tr_b16 v[218:219], v122 offset:2048\n"
"s_waitcnt lgkmcnt(0)\n"
"v_xor_b32 v122, 128, v115\n"
"v_add_u32 v122, 114688, v122\n"
"ds_read_b64_tr_b16 v[224:225], v122 offset:0\n"
"ds_read_b64_tr_b16 v[226:227], v122 offset:2048\n"
"v_xor_b32 v122, 160, v115\n"
"v_add_u32 v122, 114688, v122\n"
"ds_read_b64_tr_b16 v[232:233], v122 offset:0\n"
"ds_read_b64_tr_b16 v[234:235], v122 offset:2048\n"
"v_xor_b32 v122, 192, v115\n"
"v_add_u32 v122, 114688, v122\n"
"ds_read_b64_tr_b16 v[240:241], v122 offset:0\n"
"ds_read_b64_tr_b16 v[242:243], v122 offset:2048\n"
"v_xor_b32 v122, 224, v115\n"
"v_add_u32 v122, 114688, v122\n"
"ds_read_b64_tr_b16 v[248:249], v122 offset:0\n"
"ds_read_b64_tr_b16 v[250:251], v122 offset:2048\n"
"s_waitcnt lgkmcnt(0)\n"
"s_waitcnt lgkmcnt(0)\n"
"s_waitcnt vmcnt(0)\n"
"s_barrier\n"
"v_xor_b32 v122, 0, v115\n"
"v_add_u32 v122, 32768, v122\n"
"ds_read_b64_tr_b16 v[196:197], v122 offset:0\n"
"ds_read_b64_tr_b16 v[198:199], v122 offset:2048\n"
"v_xor_b32 v122, 32, v115\n"
"v_add_u32 v122, 32768, v122\n"
"ds_read_b64_tr_b16 v[204:205], v122 offset:0\n"
"ds_read_b64_tr_b16 v[206:207], v122 offset:2048\n"
"v_xor_b32 v122, 64, v115\n"
"v_add_u32 v122, 32768, v122\n"
"ds_read_b64_tr_b16 v[212:213], v122 offset:0\n"
"ds_read_b64_tr_b16 v[214:215], v122 offset:2048\n"
"v_xor_b32 v122, 96, v115\n"
"v_add_u32 v122, 32768, v122\n"
"ds_read_b64_tr_b16 v[220:221], v122 offset:0\n"
"ds_read_b64_tr_b16 v[222:223], v122 offset:2048\n"
"s_waitcnt lgkmcnt(0)\n"
"v_xor_b32 v122, 128, v115\n"
"v_add_u32 v122, 32768, v122\n"
"ds_read_b64_tr_b16 v[228:229], v122 offset:0\n"
"ds_read_b64_tr_b16 v[230:231], v122 offset:2048\n"
"v_xor_b32 v122, 160, v115\n"
"v_add_u32 v122, 32768, v122\n"
"ds_read_b64_tr_b16 v[236:237], v122 offset:0\n"
"ds_read_b64_tr_b16 v[238:239], v122 offset:2048\n"
"v_xor_b32 v122, 192, v115\n"
"v_add_u32 v122, 32768, v122\n"
"ds_read_b64_tr_b16 v[244:245], v122 offset:0\n"
"ds_read_b64_tr_b16 v[246:247], v122 offset:2048\n"
"v_xor_b32 v122, 224, v115\n"
"v_add_u32 v122, 32768, v122\n"
"ds_read_b64_tr_b16 v[252:253], v122 offset:0\n"
"ds_read_b64_tr_b16 v[254:255], v122 offset:2048\n"
"s_waitcnt lgkmcnt(0)\n"
"s_waitcnt lgkmcnt(0)\n"
"s_barrier\n"
"v_bfe_u32 v108, v0, 4, 1\n"
"v_mul_u32_u24 v108, 0xffff, v108\n"
"v_add_u32 v108, 1, v108\n"
"v_mul_u32_u24 v108, 0x3f80, v108\n"
"v_bfe_u32 v109, v0, 5, 2\n"
"v_cmp_eq_u32 vcc, 0, v109\n"
"s_nop 1\n"
"v_cndmask_b32 v104, 0, v108, vcc\n"
"v_cmp_eq_u32 vcc, 1, v109\n"
"s_nop 1\n"
"v_cndmask_b32 v105, 0, v108, vcc\n"
"v_cmp_eq_u32 vcc, 2, v109\n"
"s_nop 1\n"
"v_cndmask_b32 v106, 0, v108, vcc\n"
"v_cmp_eq_u32 vcc, 3, v109\n"
"s_nop 1\n"
"v_cndmask_b32 v107, 0, v108, vcc\n"
"ds_write_b128 v0, v[104:107] offset:17664\n"
"v_mov_b32 v108, 17536\n"
"ds_write_b64 v108, v[22:23]\n"
"ds_write_b64 v108, v[22:23] offset:8\n"
"s_mov_b32 s49, 0\n"
"s_mov_b32 s42, 0\n"
"s_mov_b32 s45, 0\n"
"v_lshrrev_b32 v4, 1, v0\n"
"CH_PARW:\n"
"global_load_dwordx4 v[108:111], v21, s[38:39] sc1\n"
"global_load_dwordx2 v[2:3], v4, s[38:39] offset:256 sc1\n"
"global_load_dwordx2 v[106:107], v0, s[8:9] sc1\n"
"s_add_u32 s45, s45, 1\n"
"s_waitcnt vmcnt(0)\n"
"v_cmp_ne_u64 s[16:17], %[tag], v[106:107]\n"
"v_and_b32 v104, 1, v108\n"
"v_and_b32 v105, 1, v110\n"
"v_and_b32 v108, -2, v108\n"
"v_and_b32 v110, -2, v110\n"
"v_cmp_ne_u64 vcc, %[tag], v[2:3]\n"
"v_cmp_ne_u64 s[12:13], %[tag], v[108:109]\n"
"v_cmp_ne_u64 s[14:15], %[tag], v[110:111]\n"
"v_lshl_or_b32 v104, v105, 1, v104\n"
"s_and_b64 s[16:17], s[16:17], 7\n"
"s_or_b64 s[12:13], s[12:13], s[16:17]\n"
"s_or_b64 s[12:13], s[12:13], s[14:15]\n"
"s_or_b64 s[12:13], s[12:13], vcc\n"
"s_cbranch_scc0 CH_PAROK\n"
"s_sleep 2\n"
"s_cmp_lt_u32 s45, 0x8000\n"
"s_cbranch_scc1 CH_PARW\n"
"s_mov_b32 s54, 6\n"
"CH_PAROK:\n"
"v_readfirstlane_b32 s44, v104\n"
"s_nop 3\n"
"s_lshr_b32 s45, s44, 1\n"
"s_lshl_b32 s45, s45, 13\n"
"s_add_u32 s45, s45, 0x300000\n"
"s_add_u32 s38, s36, s45\n"
"s_addc_u32 s39, s37, 0\n"
"s_and_b32 s44, s44, 1\n"
"s_lshl_b32 s44, s44, 13\n"
"s_add_u32 s36, s36, s44\n"
"s_addc_u32 s37, s37, 0\n"
"s_sub_u32 s55, s38, s36\n"
"s_sub_u32 s55, s55, 0x100000\n"
"s_barrier\n"
"s_mov_b32 m0, s89\n"
"s_nop 0\n"
"global_load_lds_dwordx4 v0, s[46:47] offset:0 sc1\n"
"global_load_lds_dwordx4 v0, s[46:47] offset:1024 sc1\n"
"global_load_lds_dwordx4 v0, s[46:47] offset:2048 sc1\n"
"s_add_u32 s46, s46, 0x4000\n"
"s_addc_u32 s47, s47, 0\n"
"s_add_u32 m0, s89, 12288\n"
"s_nop 0\n"
"global_load_lds_dwordx4 v0, s[46:47] offset:0 sc1\n"
"global_load_lds_dwordx4 v0, s[46:47] offset:1024 sc1\n"
"global_load_lds_dwordx4 v0, s[46:47] offset:2048 sc1\n"
"s_mov_b32 s49, 12288\n"
"s_add_u32 s56, s36, 0\n"
"s_addc_u32 s57, s37, 0\n"
"s_add_u32 s58, s36, 131072\n"
"s_addc_u32 s59, s37, 0\n"
"s_add_u32 s60, s36, 262144\n"
"s_addc_u32 s61, s37, 0\n"
"s_add_u32 s62, s36, 393216\n"
"s_addc_u32 s63, s37, 0\n"
"s_add_u32 s64, s36, 524288\n"
"s_addc_u32 s65, s37, 0\n"
"s_add_u32 s66, s36, 655360\n"
"s_addc_u32 s67, s37, 0\n"
"s_add_u32 s68, s36, 786432\n"
"s_addc_u32 s69, s37, 0\n"
"s_add_u32 s70, s36, 917504\n"
"s_addc_u32 s71, s37, 0\n"
"s_lshl_b32 s44, s40, 14\n"
"s_lshl_b32 s45, s41, 11\n"
"s_add_u32 s44, s44, s45\n"
"s_add_u32 s74, s36, s44\n"
"s_addc_u32 s75, s37, 0\n"
"s_lshl_b32 s43, s43, 14\n"
"s_mov_b32 s42, 0\n"
"s_add_u32 s46, s46, 0x4000\n"
"s_addc_u32 s47, s47, 0\n"
"s_mov_b32 m0, s89\n"
"s_mov_b32 s72, s55\n"
"v_mov_b64 v[96:97], -1\n"
"v_mov_b64 v[98:99], -1\n"
"s_lshl_b32 s44, s41, 18\n"
"s_add_u32 s44, s44, s72\n"
"v_add_u32 v100, s44, v10\n"
"s_cmp_gt_u32 s42, 1\n"
"s_cselect_b32 s44, 0x1000, 0\n"
"s_add_u32 s50, s50, s44\n"
"s_addc_u32 s51, s51, 0\n"
"s_add_u32 s34, s42, 3\n"
"s_and_b32 s34, s34, 3\n"
"s_lshl_b32 s34, s34, 16\n"
"s_add_u32 s90, s38, s34\n"
"s_addc_u32 s91, s39, 0\n"
"v_mov_b32 v101, v11\n"
"v_mov_b64 v[102:103], -1\n"
"s_mov_b64 s[98:99], -1\n"
"s_cmp_eq_u32 s41, 0\n"
"s_cbranch_scc1 CH_PRF\n"
"s_cmp_eq_u32 s41, 1\n"
"s_cbranch_scc0 CH_PQF\n"
"s_add_u32 s90, s90, 0x80000\n"
"s_addc_u32 s91, s91, 0\n"
"v_lshlrev_b32 v101, 10, v0\n"
"s_lshl_b32 s44, s40, 4\n"
"v_add_u32 v101, s44, v101\n"
"s_mov_b64 s[98:99], 15\n"
"s_branch CH_PRF\n"
"CH_PQF:\n"
"s_mov_b64 s[90:91], s[52:53]\n"
"v_mov_b32 v101, 0\n"
"s_sub_u32 s44, s42, 1\n"
"s_max_i32 s44, s44, 0\n"
"s_mov_b32 s45, 0\n"
"s_or_b64 s[44:45], s[44:45], %[tag]\n"
"v_mov_b32 v102, s44\n"
"v_mov_b32 v103, s45\n"
"s_cmp_eq_u32 s41, 2\n"
"s_cselect_b32 s98, 1, 0\n"
"s_mov_b32 s99, 0\n"
"CH_PRF:\n"
"s_mov_b32 s72, 0\n"
"s_mov_b32 s73, 0x100000\n"
"s_mov_b32 s95, 0\n"
"s_load_dword s28, %[par], 156\n"
"s_mov_b32 s30, 0\n"
"s_mov_b32 s31, 0\n"
"s_waitcnt lgkmcnt(0)\n"
"s_bfe_u32 s28, s28, 0x80008\n"
"s_bitcmp0_b32 s40, 3\n"
"s_cselect_b32 s28, s28, 0\n"
"s_memtime s[12:13]\n"
"s_nop 3\n"
"s_memrealtime s[14:15]\n"
"s_nop 3\n"
"s_memrealtime s[16:17]\n"
"s_nop 3\n"
"s_memrealtime s[18:19]\n"
"s_nop 3\n"
"s_memrealtime s[20:21]\n"
"s_nop 3\n"
"s_memrealtime s[22:23]\n"
"s_nop 3\n"
"s_memrealtime s[24:25]\n"
"s_nop 3\n"
"s_waitcnt lgkmcnt(0)\n"
"s_mov_b32 s13, 0\n"
"s_cmp_eq_u32 s14, s16\n"
"s_addc_u32 s13, s13, 0\n"
"s_cmp_eq_u32 s14, s18\n"
"s_addc_u32 s13, s13, 0\n"
"s_cmp_eq_u32 s14, s20\n"
"s_addc_u32 s13, s13, 0\n"
"s_cmp_eq_u32 s14, s22\n"
"s_addc_u32 s13, s13, 0\n"
"s_cmp_eq_u32 s14, s24\n"
"s_addc_u32 s13, s13, 0\n"
"s_mul_i32 s15, s13, 20\n"
"s_add_u32 s12, s12, s15\n"
"s_add_u32 s14, s14, 4\n"
"s_mul_i32 s14, s14, 24\n"
"s_sub_u32 s26, s12, s14\n"
"s_memtime s[44:45]\n"
"s_waitcnt lgkmcnt(0)\n"
"s_sub_u32 s27, s44, s26\n"
"s_sub_u32 s27, s27, 4096\n"
"s_sub_u32 s86, s86, s26\n"
"s_max_i32 s86, s86, 0\n"
"s_sub_u32 s87, s44, s87\n"
"v_cvt_f32_u32 v110, s86\n"
"v_cvt_f32_u32 v111, s87\n"
"v_rcp_f32 v111, v111\n"
"v_mul_f32 v110, 0x45834000, v110\n"
"v_mul_f32 v110, v110, v111\n"
"v_cvt_u32_f32 v110, v110\n"
"s_nop 0\n"
"v_readfirstlane_b32 s86, v110\n"
"s_add_u32 s86, s86, 60\n"
"s_min_u32 s86, s86, 2000\n"
"s_mul_i32 s83, s86, 0x3000a\n"
"s_lshr_b32 s83, s83, 4\n"
"s_and_b32 s83, s83, 0x0fff0fff\n"
"s_add_u32 s44, s27, 6996\n"
"s_add_u32 s44, s44, s86\n"
"s_and_b32 s45, s84, 0xffff\n"
"s_sub_u32 s44, s44, s45\n"
"s_sub_u32 s44, s44, s82\n"
"s_andn2_b32 s44, s44, 1\n"
"v_mov_b32 v110, s44\n"
"v_mov_b32 v111, s83\n"
"v_lshlrev_b32 v3, 10, v0\n"
"s_lshl_b32 s44, s40, 4\n"
"v_add_u32 v3, s44, v3\n"
"s_add_u32 s76, s38, 0x80000\n"
"s_addc_u32 s77, s39, 0\n"
"s_cmp_eq_u32 s41, 3\n"
"s_cselect_b32 s83, s86, s83\n"
"s_mov_b32 s10, 0\n"
"s_cselect_b32 exec_lo, 15, 0\n"
"s_mov_b32 exec_hi, 0\n"
"global_store_dwordx2 v3, v[110:111], s[76:77] sc1\n"
"s_mov_b64 exec, -1\n"
"s_cmp_eq_u32 s41, 0\n"
"s_cselect_b32 s44, 4, 0\n"
"s_add_u32 s44, s44, s43\n"
"s_add_u32 s44, s44, 0x7fffc\n"
"s_add_u32 s96, s38, s44\n"
"s_addc_u32 s97, s39, 0\n"
"s_cmp_eq_u32 s41, 1\n"
"s_cselect_b32 s96, s8, s96\n"
"s_cselect_b32 s97, s9, s97\n"
"v_min_u32 v32, -2, v32\n"
"v_min_u32 v33, -2, v33\n"
"v_min_u32 v34, -2, v34\n"
"v_min_u32 v35, -2, v35\n"
"v_min_u32 v36, -2, v36\n"
"v_min_u32 v37, -2, v37\n"
"v_min_u32 v38, -2, v38\n"
"v_min_u32 v39, -2, v39\n"
"v_mov_b64 v[112:113], 0\n"
"v_mov_b64 v[114:115], 0\n"
"v_mov_b64 v[116:117], 0\n"
"v_mov_b64 v[118:119], 0\n"
"v_mov_b64 v[120:121], 0\n"
"v_mov_b64 v[122:123], 0\n"
"v_mov_b64 v[124:125], 0\n"
"v_mov_b64 v[126:127], 0\n"
"v_mov_b64 v[28:29], 0\n"
"v_mov_b64 v[30:31], 0\n"
"s_waitcnt vmcnt(1)\n"
"s_barrier\n"
"s_mov_b32 s49, 0\n"
"v_add_u32 v3, s49, v0\n"
"v_add_u32 v4, s29, v3\n"
"v_add_u32 v3, s48, v3\n"
"ds_read_b128 v[64:67], v4 offset:19456\n"
"ds_read_b128 v[68:71], v4 offset:20480\n"
"ds_read_b128 v[72:75], v4 offset:21504\n"
"ds_read_b128 v[76:79], v4 offset:22528\n"
"ds_read_b128 v[80:83], v4 offset:23552\n"
"ds_read_b128 v[84:87], v4 offset:24576\n"
"ds_read_b128 v[88:91], v4 offset:25600\n"
"ds_read_b128 v[92:95], v4 offset:26624\n"
"ds_read_b128 v[24:27], v3\n"
"s_xor_b32 s49, s49, 12288\n"
"s_mov_b32 s30, 0\n"
"s_waitcnt lgkmcnt(0)\n"
"s_branch CH_PH_m\n"
".p2align 8\n"
"s_nop 0\n"
"s_nop 0\n"
"CH_STEP:\n"
"s_mov_b32 s7, 0x7fffffff\n"
"s_mov_b32 s30, 0\n"
"CH_PA_m:\n"
"global_load_dwordx4 v[32:35], v0, s[78:79] sc1\n"
"global_load_dwordx4 v[36:39], v0, s[78:79] offset:1024 sc1\n"
"s_cmp_eq_u32 s22, 0\n"
"s_cbranch_scc1 CH_PT_m\n"
"s_add_u32 s44, s21, s85\n"
"s_sub_u32 s44, s44, s27\n"
"s_cmp_gt_u32 s31, 1\n"
"s_cselect_b32 s44, -1, s44\n"
"s_cselect_b32 s16, s18, s19\n"
"s_cmp_gt_u32 s20, 1\n"
"s_cselect_b32 s44, 1, s44\n"
"s_cmp_gt_i32 s44, 0\n"
"s_cselect_b32 s17, s18, s19\n"
"s_cmp_lt_u32 s42, 4\n"
"s_cselect_b32 s16, 0, s16\n"
"s_cselect_b32 s17, 0, s17\n"
"s_and_b32 s44, s10, 0xffff\n"
"s_add_i32 s44, s44, s16\n"
"s_max_i32 s44, s44, 0\n"
"s_min_i32 s44, s44, 800\n"
"s_lshr_b32 s45, s10, 16\n"
"s_add_i32 s45, s45, s17\n"
"s_max_i32 s45, s45, 0\n"
"s_min_i32 s45, s45, 800\n"
"s_lshl_b32 s45, s45, 16\n"
"s_or_b32 s10, s44, s45\n"
"s_mul_i32 s44, s83, 0x3000a\n"
"s_lshr_b32 s44, s44, 4\n"
"s_and_b32 s44, s44, 0x0fff0fff\n"
"s_add_u32 s44, s44, s10\n"
"v_mov_b32 v111, s44\n"
"CH_PT_m:\n"
"s_mov_b32 exec_lo, s22\n"
"s_mov_b32 exec_hi, 0\n"
"global_store_dwordx2 v3, v[110:111], s[76:77] sc1\n"
"s_mov_b64 exec, -1\n"
"s_mov_b32 s22, 0\n"
"CH_PH_m:\n"
"s_add_u32 s30, s30, 1\n"
"s_waitcnt vmcnt(2)\n"
"ds_write_b128 v1, v[32:35]\n"
"v_smfmac_f32_16x16x64_bf16 v[112:115], v[32:35], a[0:7], v9\n"
"v_max3_u32 v4, v32, v33, v34\n"
"v_smfmac_f32_16x16x64_bf16 v[116:119], v[32:35], a[64:71], v9\n"
"s_waitcnt vmcnt(1)\n"
"ds_write_b128 v1, v[36:39] offset:1024\n"
"s_mov_b32 s7, 0x7fffffff\n"
"v_smfmac_f32_16x16x64_bf16 v[120:123], v[32:35], a[128:135], v9\n"
"v_max3_u32 v3, v35, v36, v37\n"
"v_smfmac_f32_16x16x64_bf16 v[124:127], v[32:35], a[192:199], v9\n"
"v_max3_u32 v4, v38, v39, v4\n"
"v_smfmac_f32_16x16x64_bf16 v[112:115], v[36:39], a[8:15], v9\n"
"v_max_u32 v4, v3, v4\n"
"v_smfmac_f32_16x16x64_bf16 v[116:119], v[36:39], a[72:79], v9\n"
"v_cmp_eq_u32 vcc, -1, v4\n"
"v_smfmac_f32_16x16x64_bf16 v[120:123], v[36:39], a[136:143], v9\n"
"v_xor_b32 v4, 0x800, v1\n"
"s_cbranch_vccnz CH_PF_m\n"
"s_waitcnt lgkmcnt(0)\n"
"s_barrier\n"
"ds_read_b128 v[40:43], v4\n"
"ds_read_b128 v[44:47], v4 offset:1024\n"
"v_xor_b32 v3, 0x1000, v1\n"
"v_smfmac_f32_16x16x64_bf16 v[124:127], v[36:39], a[200:207], v9\n"
"ds_read_b128 v[48:51], v3\n"
"ds_read_b128 v[52:55], v3 offset:1024\n"
"v_xor_b32 v4, 0x1800, v1\n"
"ds_read_b128 v[56:59], v4\n"
"ds_read_b128 v[60:63], v4 offset:1024\n"
"global_load_dwordx2 v[2:3], v0, s[96:97] sc1\n"
"s_waitcnt lgkmcnt(5)\n"
"v_smfmac_f32_16x16x64_bf16 v[112:115], v[40:43], a[16:23], v9\n"
"s_add_u32 s76, s74, s72\n"
"s_addc_u32 s77, s75, 0\n"
"v_smfmac_f32_16x16x64_bf16 v[116:119], v[40:43], a[80:87], v9\n"
"s_add_u32 s44, s42, 1\n"
"s_and_b32 s44, s44, 3\n"
"s_waitcnt lgkmcnt(4)\n"
"v_smfmac_f32_16x16x64_bf16 v[112:115], v[44:47], a[24:31], v9\n"
"s_lshl_b32 s44, s44, 16\n"
"s_add_u32 s80, s38, s44\n"
"global_load_lds_dwordx4 v0, s[46:47] offset:0 sc1\n"
"v_smfmac_f32_16x16x64_bf16 v[116:119], v[44:47], a[88:95], v9\n"
"s_addc_u32 s81, s39, 0\n"
"s_add_u32 s78, s80, s43\n"
"s_waitcnt lgkmcnt(3)\n"
"v_smfmac_f32_16x16x64_bf16 v[112:115], v[48:51], a[32:39], v9\n"
"s_addc_u32 s79, s81, 0\n"
"s_sub_u32 s34, s27, s85\n"
"v_smfmac_f32_16x16x64_bf16 v[116:119], v[48:51], a[96:103], v9\n"
"s_waitcnt lgkmcnt(2)\n"
"v_smfmac_f32_16x16x64_bf16 v[112:115], v[52:55], a[40:47], v9\n"
"global_store_dwordx4 v0, v[12:15], s[50:51] sc1\n"
"v_smfmac_f32_16x16x64_bf16 v[116:119], v[52:55], a[104:111], v9\n"
"s_waitcnt lgkmcnt(1)\n"
"v_smfmac_f32_16x16x64_bf16 v[112:115], v[56:59], a[48:55], v9\n"
"v_smfmac_f32_16x16x64_bf16 v[116:119], v[56:59], a[112:119], v9\n"
"s_waitcnt lgkmcnt(0)\n"
"v_smfmac_f32_16x16x64_bf16 v[112:115], v[60:63], a[56:63], v9\n"
"global_load_lds_dwordx4 v0, s[46:47] offset:1024 sc1\n"
"v_smfmac_f32_16x16x64_bf16 v[116:119], v[60:63], a[120:127], v9\n"
"v_smfmac_f32_16x16x64_bf16 v[120:123], v[40:43], a[144:151], v9\n"
"s_nop 2\n"
"v_smfmac_f32_16x16x64_bf16 v[124:127], v[40:43], a[208:215], v9\n"
"s_nop 1\n"
"v_add_f32 v112, v112, v114\n"
"v_smfmac_f32_16x16x64_bf16 v[120:123], v[44:47], a[152:159], v9\n"
"v_add_f32 v113, v113, v115\n"
"global_store_dwordx4 v100, v[96:99], s[56:57] sc1\n"
"v_smfmac_f32_16x16x64_bf16 v[124:127], v[44:47], a[216:223], v9\n"
"v_add_f32 v116, v116, v118\n"
"v_smfmac_f32_16x16x64_bf16 v[120:123], v[48:51], a[160:167], v9\n"
"v_add_f32 v117, v117, v119\n"
"v_smfmac_f32_16x16x64_bf16 v[124:127], v[48:51], a[224:231], v9\n"
"v_mul_f32 v112, s88, v112\n"
"v_smfmac_f32_16x16x64_bf16 v[120:123], v[52:55], a[168:175], v9\n"
"v_mul_f32 v113, s88, v113\n"
"global_load_lds_dwordx4 v0, s[46:47] offset:2048 sc1\n"
"v_smfmac_f32_16x16x64_bf16 v[124:127], v[52:55], a[232:239], v9\n"
"v_mul_f32 v116, s88, v116\n"
"v_smfmac_f32_16x16x64_bf16 v[120:123], v[56:59], a[176:183], v9\n"
"v_mul_f32 v117, s88, v117\n"
"v_smfmac_f32_16x16x64_bf16 v[124:127], v[56:59], a[240:247], v9\n"
"v_cvt_pk_bf16_f32 v112, v112, v113\n"
"v_smfmac_f32_16x16x64_bf16 v[120:123], v[60:63], a[184:191], v9\n"
"v_cvt_pk_bf16_f32 v116, v116, v117\n"
"v_smfmac_f32_16x16x64_bf16 v[124:127], v[60:63], a[248:255], v9\n"
"ds_write2_b32 v5, v112, v116 offset0:1 offset1:193\n"
"v_smfmac_f32_16x16x64_bf16 v[28:31], v[32:35], v[128:135], v9\n"
"s_nop 2\n"
"v_smfmac_f32_16x16x64_bf16 v[28:31], v[36:39], v[136:143], v9\n"
"s_nop 1\n"
"v_add_f32 v120, v120, v122\n"
"v_add_f32 v121, v121, v123\n"
"v_add_f32 v124, v124, v126\n"
"v_add_f32 v125, v125, v127\n"
"v_cvt_pk_bf16_f32 v12, v120, v121\n"
"v_cvt_pk_bf16_f32 v13, v124, v125\n"
"ds_write2_b32 v5, v12, v13 offset0:0 offset1:192\n"
"v_smfmac_f32_16x16x64_bf16 v[28:31], v[40:43], v[144:151], v9\n"
"s_mov_b64 exec, s[98:99]\n"
"global_store_dwordx2 v101, v[102:103], s[90:91] sc1\n"
"s_mov_b64 exec, -1\n"
"v_smfmac_f32_16x16x64_bf16 v[28:31], v[44:47], v[152:159], v9\n"
"v_smfmac_f32_16x16x64_bf16 v[28:31], v[48:51], v[160:167], v9\n"
"v_smfmac_f32_16x16x64_bf16 v[28:31], v[52:55], v[168:175], v9\n"
"global_store_dwordx4 v100, v[96:99], s[58:59] sc1\n"
"s_waitcnt lgkmcnt(0)\n"
"s_barrier\n"
"ds_read_b64_tr_b16 v[96:97], v6 offset:0\n"
"ds_read_b64_tr_b16 v[98:99], v6 offset:128\n"
"ds_read_b64_tr_b16 v[100:101], v6 offset:1536\n"
"ds_read_b64_tr_b16 v[102:103], v6 offset:1664\n"
"v_smfmac_f32_16x16x64_bf16 v[28:31], v[56:59], v[176:183], v9\n"
"ds_read_b64_tr_b16 v[104:105], v6 offset:3072\n"
"ds_read_b64_tr_b16 v[106:107], v6 offset:3200\n"
"v_smfmac_f32_16x16x64_bf16 v[28:31], v[60:63], v[184:191], v9\n"
"ds_read_b64_tr_b16 v[108:109], v6 offset:4608\n"
"ds_read_b64_tr_b16 v[110:111], v6 offset:4736\n"
"s_waitcnt lgkmcnt(6)\n"
"v_mfma_f32_16x16x32_bf16 v[120:123], v[96:99], v[96:99], 0\n"
"s_waitcnt lgkmcnt(4)\n"
"v_mfma_f32_16x16x32_bf16 v[120:123], v[100:103], v[100:103], v[120:123]\n"
"s_waitcnt lgkmcnt(2)\n"
"v_mfma_f32_16x16x32_bf16 v[120:123], v[104:107], v[104:107], v[120:123]\n"
"s_waitcnt lgkmcnt(0)\n"
"v_mfma_f32_16x16x32_bf16 v[120:123], v[108:111], v[108:111], v[120:123]\n"
"v_mfma_f32_16x16x32_bf16 v[112:115], v[64:67], v[96:99], 0\n"
"v_add_f32 v28, v28, v30\n"
"v_add_f32 v29, v29, v31\n"
"v_mfma_f32_16x16x32_bf16 v[112:115], v[68:71], v[100:103], v[112:115]\n"
"s_nop 1\n"
"v_cvt_pk_bf16_f32 v14, v28, v29\n"
"v_mfma_f32_16x16x32_bf16 v[112:115], v[72:75], v[104:107], v[112:115]\n"
"v_exp_f32 v72, v120\n"
"v_mfma_f32_16x16x32_bf16 v[112:115], v[76:79], v[108:111], v[112:115]\n"
"v_exp_f32 v73, v121\n"
"v_mfma_f32_16x16x32_bf16 v[116:119], v[80:83], v[96:99], 0\n"
"v_max3_f32 v75, abs(v120), abs(v121), abs(v121)\n"
"v_cvt_pk_bf16_f32 v20, v72, v73\n"
"v_mfma_f32_16x16x32_bf16 v[116:119], v[84:87], v[100:103], v[116:119]\n"
"s_nop 2\n"
"v_mfma_f32_16x16x32_bf16 v[116:119], v[88:91], v[104:107], v[116:119]\n"
"v_exp_f32 v64, v112\n"
"v_mfma_f32_16x16x32_bf16 v[116:119], v[92:95], v[108:111], v[116:119]\n"
"v_exp_f32 v65, v113\n"
"v_mfma_f32_16x16x32_bf16 v[80:83], v[16:19], v[20:23], 0\n"
"v_exp_f32 v66, v114\n"
"v_mfma_f32_16x16x32_bf16 v[124:127], v[14:17], v[20:23], 0\n"
"v_exp_f32 v67, v115\n"
"v_cvt_pk_bf16_f32 v84, v64, v65\n"
"v_max3_f32 v74, abs(v112), abs(v113), abs(v114)\n"
"v_cvt_pk_bf16_f32 v85, v66, v67\n"
"v_exp_f32 v68, v116\n"
"v_exp_f32 v69, v117\n"
"v_exp_f32 v70, v118\n"
"v_exp_f32 v71, v119\n"
"v_cvt_pk_bf16_f32 v86, v68, v69\n"
"v_cvt_pk_bf16_f32 v87, v70, v71\n"
"s_nop 1\n"
"v_mfma_f32_16x16x32_bf16 v[80:83], v[16:19], v[84:87], v[80:83]\n"
"v_mfma_f32_16x16x32_bf16 v[124:127], v[24:27], v[84:87], v[124:127]\n"
"s_mov_b32 exec_lo, 0xcccccccc\n"
"s_mov_b32 exec_hi, 0xcccccccc\n"
"v_max3_f32 v75, abs(v115), abs(v116), v75\n"
"v_max3_f32 v74, abs(v117), abs(v118), v74\n"
"v_max3_f32 v74, abs(v119), v75, v74\n"
"v_cmp_nge_f32 vcc, 0x42200000, v74\n"
"CH_JOIN:\n"
"s_nop 0\n"
"v_rcp_f32 v80, v80\n"
"s_nop 0\n"
"v_pk_mul_f32 v[76:77], v[124:125], v[80:81] op_sel_hi:[1,0]\n"
"v_pk_mul_f32 v[78:79], v[126:127], v[80:81] op_sel_hi:[1,0]\n"
"v_cvt_pk_bf16_f32 v76, v76, v77\n"
"v_cvt_pk_bf16_f32 v77, v78, v79\n"
"s_cbranch_vccnz CH_SLOW\n"
"ds_write_b64 v7, v[76:77]\n"
"s_mov_b64 exec, -1\n"
"s_waitcnt vmcnt(7)\n"
"v_cmp_eq_u32 vcc, -1, v3\n"
"v_subrev_u32 v4, s34, v3\n"
"v_mov_b64 v[32:33], 0\n"
"v_mov_b64 v[34:35], 0\n"
"v_mov_b64 v[36:37], 0\n"
"v_mov_b64 v[38:39], 0\n"
"v_mov_b64 v[40:41], 0\n"
"v_mov_b64 v[42:43], 0\n"
"v_mov_b64 v[44:45], 0\n"
"v_mov_b64 v[46:47], 0\n"
"v_cmp_lt_i32 s[44:45], 0, v4\n"
"s_waitcnt lgkmcnt(0)\n"
"s_barrier\n"
"ds_read_b128 v[64:67], v8\n"
"ds_read_b128 v[104:107], v0 offset:17664\n"
"v_mov_b64 v[48:49], 0\n"
"v_mov_b64 v[50:51], 0\n"
"v_mov_b64 v[52:53], 0\n"
"v_mov_b64 v[54:55], 0\n"
"v_mov_b64 v[56:57], 0\n"
"v_mov_b64 v[58:59], 0\n"
"v_mov_b64 v[60:61], 0\n"
"v_mov_b64 v[62:63], 0\n"
"s_or_b64 s[44:45], s[44:45], vcc\n"
"s_cselect_b32 s97, 1, 0\n"
"s_waitcnt lgkmcnt(1)\n"
"v_smfmac_f32_16x16x64_bf16 v[32:35], v[64:67], v[192:199], v9\n"
"s_add_u32 s87, s27, s82\n"
"s_mov_b32 s86, s26\n"
"s_nop 0\n"
"v_smfmac_f32_16x16x64_bf16 v[36:39], v[64:67], v[200:207], v9\n"
"s_cmp_lt_u32 s42, 1\n"
"s_cselect_b32 s97, 1, s97\n"
"s_memtime s[12:13]\n"
"v_smfmac_f32_16x16x64_bf16 v[40:43], v[64:67], v[208:215], v9\n"
"s_memrealtime s[14:15]\n"
"v_add_f32 v32, v32, v34\n"
"v_smfmac_f32_16x16x64_bf16 v[44:47], v[64:67], v[216:223], v9\n"
"s_memrealtime s[16:17]\n"
"v_add_f32 v33, v33, v35\n"
"v_smfmac_f32_16x16x64_bf16 v[48:51], v[64:67], v[224:231], v9\n"
"s_memrealtime s[18:19]\n"
"v_cvt_pk_bf16_f32 v96, v32, v33\n"
"v_smfmac_f32_16x16x64_bf16 v[52:55], v[64:67], v[232:239], v9\n"
"s_memrealtime s[20:21]\n"
"v_add_f32 v36, v36, v38\n"
"v_smfmac_f32_16x16x64_bf16 v[56:59], v[64:67], v[240:247], v9\n"
"s_memrealtime s[22:23]\n"
"v_add_f32 v37, v37, v39\n"
"v_smfmac_f32_16x16x64_bf16 v[60:63], v[64:67], v[248:255], v9\n"
"s_memrealtime s[24:25]\n"
"v_cvt_pk_bf16_f32 v97, v36, v37\n"
"v_add_f32 v40, v40, v42\n"
"v_add_f32 v41, v41, v43\n"
"v_cvt_pk_bf16_f32 v98, v40, v41\n"
"v_add_f32 v44, v44, v46\n"
"v_add_f32 v45, v45, v47\n"
"v_cvt_pk_bf16_f32 v99, v44, v45\n"
"global_store_dwordx4 v0, v[96:99], s[76:77] sc1\n"
"v_pk_add_f32 v[48:49], v[48:49], v[50:51]\n"
"v_pk_add_f32 v[52:53], v[52:53], v[54:55]\n"
"v_pk_add_f32 v[56:57], v[56:57], v[58:59]\n"
"v_pk_add_f32 v[60:61], v[60:61], v[62:63]\n"
"v_cvt_pk_bf16_f32 v100, v48, v49\n"
"v_cvt_pk_bf16_f32 v101, v52, v53\n"
"v_cvt_pk_bf16_f32 v102, v56, v57\n"
"v_cvt_pk_bf16_f32 v103, v60, v61\n"
"global_store_dwordx4 v0, v[100:103], s[76:77] offset:1024 sc1\n"
"s_mov_b32 s7, 0x7fffffff\n"
"s_memtime s[44:45]\n"
"s_add_u32 s42, s42, 1\n"
"s_cmp_eq_u32 s41, 3\n"
"s_cbranch_scc1 CH_RED\n"
"v_max3_u32 v30, v96, v97, v98\n"
"v_max3_u32 v31, v99, v100, v101\n"
"v_max3_u32 v30, v102, v103, v30\n"
"v_max_u32 v30, v30, v31\n"
"v_cmp_eq_u32 vcc, -1, v30\n"
"s_cbranch_vccz CH_PGW\n"
"v_min_u32 v96, -2, v96\n"
"v_min_u32 v97, -2, v97\n"
"v_min_u32 v98, -2, v98\n"
"v_min_u32 v99, -2, v99\n"
"v_min_u32 v100, -2, v100\n"
"v_min_u32 v101, -2, v101\n"
"v_min_u32 v102, -2, v102\n"
"v_min_u32 v103, -2, v103\n"
"global_store_dwordx4 v0, v[96:99], s[76:77] sc1\n"
"global_store_dwordx4 v0, v[100:103], s[76:77] offset:1024 sc1\n"
"CH_PGW:\n"
"s_waitcnt lgkmcnt(0)\n"
"s_mov_b32 s13, 0\n"
"s_cmp_eq_u32 s14, s16\n"
"s_addc_u32 s13, s13, 0\n"
"s_cmp_eq_u32 s14, s18\n"
"s_addc_u32 s13, s13, 0\n"
"s_cmp_eq_u32 s14, s20\n"
"s_addc_u32 s13, s13, 0\n"
"s_cmp_eq_u32 s14, s22\n"
"s_addc_u32 s13, s13, 0\n"
"s_cmp_eq_u32 s14, s24\n"
"s_addc_u32 s13, s13, 0\n"
"s_mul_i32 s15, s13, 17\n"
"s_add_u32 s12, s12, s15\n"
"s_add_u32 s14, s14, 4\n"
"s_mul_i32 s14, s14, 24\n"
"s_sub_u32 s26, s12, s14\n"
"s_cmp_lg_u32 s41, 1\n"
"s_cbranch_scc1 CH_KVOK_W\n"
"s_add_u32 s44, s42, 2\n"
"s_min_u32 s44, s44, 63\n"
"s_mov_b32 s45, 0\n"
"s_branch CH_KVH_W\n"
"CH_KVW_W:\n"
"global_load_dwordx2 v[2:3], v0, s[8:9] sc1\n"
"s_waitcnt vmcnt(0)\n"
"CH_KVH_W:\n"
"s_add_u32 s45, s45, 1\n"
"v_cmp_ne_u64 vcc, %[tag], v[2:3]\n"
"s_ff1_i32_b64 s34, vcc\n"
"s_cmp_gt_u32 s34, s44\n"
"s_cbranch_scc1 CH_KVOK_W\n"
"s_sleep 2\n"
"s_cmp_lt_u32 s45, 0x8000\n"
"s_cbranch_scc1 CH_KVW_W\n"
"s_mov_b32 s54, 5\n"
"CH_KVOK_W:\n"
"s_cmp_lg_u32 s41, 0\n"
"s_cbranch_scc1 CH_NOREP\n"
"s_load_dwordx2 s[18:19], %[par], 96\n"
"s_mov_b32 s97, s39\n"
"s_mov_b32 s45, 0\n"
"CH_REP1:\n"
"v_cmp_eq_u32 vcc, -1, v3\n"
"s_cbranch_vccz CH_REP2\n"
"s_add_u32 s45, s45, 1\n"
"s_cmp_gt_u32 s45, 4\n"
"s_cbranch_scc1 CH_REP3\n"
"global_load_dwordx2 v[2:3], v0, s[96:97] sc1\n"
"s_waitcnt vmcnt(0)\n"
"s_branch CH_REP1\n"
"CH_REP3:\n"
"v_mov_b32 v3, s83\n"
"CH_REP2:\n"
"v_lshrrev_b32 v4, 16, v3\n"
"v_and_b32 v3, 0xffff, v3\n"
"s_nop 0\n"
"v_max_u32 v4, v4, v4 row_ror:1 row_mask:0xf bank_mask:0xf\n"
"v_max_i32 v3, v3, v3 row_ror:1 row_mask:0xf bank_mask:0xf\n"
"s_nop 0\n"
"v_max_u32 v4, v4, v4 row_ror:2 row_mask:0xf bank_mask:0xf\n"
"v_max_i32 v3, v3, v3 row_ror:2 row_mask:0xf bank_mask:0xf\n"
"s_nop 0\n"
"v_max_u32 v4, v4, v4 row_ror:4 row_mask:0xf bank_mask:0xf\n"
"v_max_i32 v3, v3, v3 row_ror:4 row_mask:0xf bank_mask:0xf\n"
"s_nop 0\n"
"v_max_u32 v4, v4, v4 row_ror:8 row_mask:0xf bank_mask:0xf\n"
"v_max_i32 v3, v3, v3 row_ror:8 row_mask:0xf bank_mask:0xf\n"
"s_nop 0\n"
"v_max_u32 v4, v4, v4 row_bcast:15 row_mask:0xa bank_mask:0xf\n"
"v_max_i32 v3, v3, v3 row_bcast:15 row_mask:0xa bank_mask:0xf\n"
"s_nop 0\n"
"v_max_u32 v4, v4, v4 row_bcast:31 row_mask:0xc bank_mask:0xf\n"
"v_max_i32 v3, v3, v3 row_bcast:31 row_mask:0xc bank_mask:0xf\n"
"s_nop 0\n"
"v_readlane_b32 s44, v3, 63\n"
"v_readlane_b32 s45, v4, 63\n"
"s_waitcnt lgkmcnt(0)\n"
"s_lshl_b32 s45, s45, 16\n"
"s_or_b32 s83, s44, s45\n"
"s_sub_u32 s19, s19, s18\n"
"s_lshl_b32 s19, s19, 16\n"
"s_or_b32 s18, s18, s19\n"
"s_add_u32 s44, s83, s18\n"
"v_mov_b32 v104, s44\n"
"v_mov_b32 v105, s42\n"
"v_mov_b32 v4, 17544\n"
"ds_write_b64 v4, v[104:105]\n"
"CH_NOREP:\n"
"v_add_u32 v3, s49, v0\n"
"v_add_u32 v4, s29, v3\n"
"v_add_u32 v3, s48, v3\n"
"ds_read_b128 v[64:67], v4 offset:19456\n"
"ds_read_b128 v[68:71], v4 offset:20480\n"
"ds_read_b128 v[72:75], v4 offset:21504\n"
"ds_read_b128 v[76:79], v4 offset:22528\n"
"ds_read_b128 v[80:83], v4 offset:23552\n"
"ds_read_b128 v[84:87], v4 offset:24576\n"
"ds_read_b128 v[88:91], v4 offset:25600\n"
"ds_read_b128 v[92:95], v4 offset:26624\n"
"ds_read_b128 v[24:27], v3\n"
"s_xor_b32 s49, s49, 12288\n"
"v_mov_b64 v[112:113], 0\n"
"v_mov_b64 v[114:115], 0\n"
"v_mov_b64 v[116:117], 0\n"
"v_mov_b64 v[118:119], 0\n"
"v_mov_b64 v[120:121], 0\n"
"v_mov_b64 v[122:123], 0\n"
"v_mov_b64 v[124:125], 0\n"
"v_mov_b64 v[126:127], 0\n"
"v_mov_b64 v[28:29], 0\n"
"v_mov_b64 v[30:31], 0\n"
"v_mov_b64 v[96:97], -1\n"
"v_mov_b64 v[98:99], -1\n"
"s_lshl_b32 s44, s41, 18\n"
"s_add_u32 s44, s44, s72\n"
"v_add_u32 v100, s44, v10\n"
"s_cmp_gt_u32 s42, 1\n"
"s_cselect_b32 s44, 0x1000, 0\n"
"s_add_u32 s50, s50, s44\n"
"s_addc_u32 s51, s51, 0\n"
"s_add_u32 s34, s42, 3\n"
"s_and_b32 s34, s34, 3\n"
"s_lshl_b32 s34, s34, 16\n"
"s_add_u32 s90, s38, s34\n"
"s_addc_u32 s91, s39, 0\n"
"v_mov_b32 v101, v11\n"
"v_mov_b64 v[102:103], -1\n"
"s_mov_b64 s[98:99], -1\n"
"s_cmp_eq_u32 s41, 0\n"
"s_cbranch_scc1 CH_PRW\n"
"s_cmp_eq_u32 s41, 1\n"
"s_cbranch_scc0 CH_PQW\n"
"s_add_u32 s90, s90, 0x80000\n"
"s_addc_u32 s91, s91, 0\n"
"v_lshlrev_b32 v101, 10, v0\n"
"s_lshl_b32 s44, s40, 4\n"
"v_add_u32 v101, s44, v101\n"
"s_mov_b64 s[98:99], 15\n"
"s_branch CH_PRW\n"
"CH_PQW:\n"
"s_mov_b64 s[90:91], s[52:53]\n"
"v_mov_b32 v101, 0\n"
"s_sub_u32 s44, s42, 1\n"
"s_max_i32 s44, s44, 0\n"
"s_mov_b32 s45, 0\n"
"s_or_b64 s[44:45], s[44:45], %[tag]\n"
"v_mov_b32 v102, s44\n"
"v_mov_b32 v103, s45\n"
"s_cmp_eq_u32 s41, 2\n"
"s_cselect_b32 s98, 1, 0\n"
"s_mov_b32 s99, 0\n"
"CH_PRW:\n"
"s_cmp_lt_u32 s42, 61\n"
"s_cselect_b32 s44, 0x4000, 0\n"
"s_add_u32 s46, s46, s44\n"
"s_addc_u32 s47, s47, 0\n"
"s_xor_b32 s44, s49, 12288\n"
"s_add_u32 m0, s89, s44\n"
"s_cmp_eq_u32 s41, 0\n"
"s_cselect_b32 s44, 4, 0\n"
"s_add_u32 s44, s44, 0x7fffc\n"
"s_add_u32 s96, s78, s44\n"
"s_addc_u32 s97, s79, 0\n"
"s_cmp_eq_u32 s41, 1\n"
"s_cselect_b32 s96, s8, s96\n"
"s_cselect_b32 s97, s9, s97\n"
"s_lshl_b32 s44, s41, 11\n"
"s_add_u32 s78, s78, s44\n"
"s_addc_u32 s79, s79, 0\n"
"s_mov_b32 s44, s55\n"
"s_mov_b32 s55, s72\n"
"s_mov_b32 s72, s73\n"
"s_mov_b32 s73, s44\n"
"v_mov_b32 v4, 17536\n"
"s_mov_b32 s31, 0\n"
"CH_AW:\n"
"ds_read_b128 v[104:107], v4\n"
"s_add_u32 s31, s31, 1\n"
"s_waitcnt lgkmcnt(0)\n"
"v_readfirstlane_b32 s44, v105\n"
"v_readfirstlane_b32 s45, v107\n"
"v_readfirstlane_b32 s27, v104\n"
"v_readfirstlane_b32 s34, v106\n"
"s_xor_b32 s44, s44, s42\n"
"s_xor_b32 s45, s45, s42\n"
"s_or_b32 s44, s44, s45\n"
"s_cbranch_scc0 CH_AW2\n"
"s_sleep 1\n"
"s_cmp_lt_u32 s31, 0x10000\n"
"s_cbranch_scc1 CH_AW\n"
"CH_AW2:\n"
"s_lshr_b32 s34, s34, 16\n"
"s_add_u32 s27, s27, s34\n"
"s_mov_b32 s22, 0\n"
"v_mov_b32 v3, 0\n"
"s_branch CH_NEXT\n"
"CH_RED:\n"
"v_add_u32 v4, s72, v10\n"
"v_max3_u32 v30, v96, v97, v98\n"
"v_max3_u32 v31, v99, v100, v101\n"
"v_max3_u32 v30, v102, v103, v30\n"
"v_max_u32 v30, v30, v31\n"
"v_cmp_eq_u32 vcc, -1, v30\n"
"s_cbranch_vccz CH_PGR\n"
"v_min_u32 v96, -2, v96\n"
"v_min_u32 v97, -2, v97\n"
"v_min_u32 v98, -2, v98\n"
"v_min_u32 v99, -2, v99\n"
"v_min_u32 v100, -2, v100\n"
"v_min_u32 v101, -2, v101\n"
"v_min_u32 v102, -2, v102\n"
"v_min_u32 v103, -2, v103\n"
"global_store_dwordx4 v0, v[96:99], s[76:77] sc1\n"
"global_store_dwordx4 v0, v[100:103], s[76:77] offset:1024 sc1\n"
"CH_PGR:\n"
"s_waitcnt lgkmcnt(0)\n"
"s_mov_b32 s13, 0\n"
"s_cmp_eq_u32 s14, s16\n"
"s_addc_u32 s13, s13, 0\n"
"s_cmp_eq_u32 s14, s18\n"
"s_addc_u32 s13, s13, 0\n"
"s_cmp_eq_u32 s14, s20\n"
"s_addc_u32 s13, s13, 0\n"
"s_cmp_eq_u32 s14, s22\n"
"s_addc_u32 s13, s13, 0\n"
"s_cmp_eq_u32 s14, s24\n"
"s_addc_u32 s13, s13, 0\n"
"s_mul_i32 s15, s13, 17\n"
"s_add_u32 s12, s12, s15\n"
"s_add_u32 s14, s14, 4\n"
"s_mul_i32 s14, s14, 24\n"
"s_sub_u32 s26, s12, s14\n"
"s_cmp_lg_u32 s97, 0\n"
"s_cbranch_scc1 CH_TSBAD\n"
"s_add_u32 s87, s87, s26\n"
"s_sub_u32 s87, s87, s28\n"
"s_add_u32 s44, s44, 170\n"
"s_branch CH_WP_G\n"
"CH_WC_G:\n"
"s_memtime s[44:45]\n"
"s_waitcnt lgkmcnt(0)\n"
"CH_WP_G:\n"
"s_sub_u32 s44, s87, s44\n"
"s_sub_u32 s44, s44, 110\n"
"s_cmp_gt_u32 s44, 16384\n"
"s_cbranch_scc1 CH_WE_G\n"
"s_cmp_gt_u32 s44, 252\n"
"s_cbranch_scc1 CH_WS_G\n"
"s_and_b32 s45, s44, 0xfc\n"
"s_getpc_b64 s[4:5]\n"
"s_add_u32 s4, s4, 276\n"
"s_addc_u32 s5, s5, 0\n"
"s_sub_u32 s4, s4, s45\n"
"s_subb_u32 s5, s5, 0\n"
"s_setpc_b64 s[4:5]\n"
"s_nop 0\n"
"s_nop 0\n"
"s_nop 0\n"
"s_nop 0\n"
"s_nop 0\n"
"s_nop 0\n"
"s_nop 0\n"
"s_nop 0\n"
"s_nop 0\n"
"s_nop 0\n"
"s_nop 0\n"
"s_nop 0\n"
"s_nop 0\n"
"s_nop 0\n"
"s_nop 0\n"
"s_nop 0\n"
"s_nop 0\n"
"s_nop 0\n"
"s_nop 0\n"
"s_nop 0\n"
"s_nop 0\n"
"s_nop 0\n"
"s_nop 0\n"
"s_nop 0\n"
"s_nop 0\n"
"s_nop 0\n"
"s_nop 0\n"
"s_nop 0\n"
"s_nop 0\n"
"s_nop 0\n"
"s_nop 0\n"
"s_nop 0\n"
"s_nop 0\n"
"s_nop 0\n"
"s_nop 0\n"
"s_nop 0\n"
"s_nop 0\n"
"s_nop 0\n"
"s_nop 0\n"
"s_nop 0\n"
"s_nop 0\n"
"s_nop 0\n"
"s_nop 0\n"
"s_nop 0\n"
"s_nop 0\n"
"s_nop 0\n"
"s_nop 0\n"
"s_nop 0\n"
"s_nop 0\n"
"s_nop 0\n"
"s_nop 0\n"
"s_nop 0\n"
"s_nop 0\n"
"s_nop 0\n"
"s_nop 0\n"
"s_nop 0\n"
"s_nop 0\n"
"s_nop 0\n"
"s_nop 0\n"
"s_nop 0\n"
"s_nop 0\n"
"s_nop 0\n"
"s_nop 0\n"
"CH_WE_G:\n"
"s_mov_b32 s7, 0x7fffffff\n"
"global_load_dwordx4 v[32:35], v4, s[56:57] sc1\n"
"global_load_dwordx4 v[36:39], v4, s[58:59] sc1\n"
"global_load_dwordx4 v[40:43], v4, s[60:61] sc1\n"
"global_load_dwordx4 v[44:47], v4, s[62:63] sc1\n"
"global_load_dwordx4 v[48:51], v4, s[64:65] sc1\n"
"global_load_dwordx4 v[52:55], v4, s[66:67] sc1\n"
"global_load_dwordx4 v[56:59], v4, s[68:69] sc1\n"
"global_load_dwordx4 v[60:63], v4, s[70:71] sc1\n"
"s_sub_u32 s27, s87, s26\n"
"s_add_u32 s27, s27, s28\n"
"v_mov_b32 v28, s27\n"
"v_mov_b32 v29, s42\n"
"v_mov_b32 v30, 17536\n"
"ds_write_b64 v30, v[28:29]\n"
"v_mov_b32 v31, v4\n"
"s_sub_u32 s16, s86, s26\n"
"s_max_i32 s16, s16, 0\n"
"s_min_i32 s16, s16, 2000\n"
"s_add_u32 s16, s16, s83\n"
"s_lshr_b32 s16, s16, 1\n"
"s_cmp_lg_u32 s97, 0\n"
"s_cselect_b32 s83, s83, s16\n"
"s_cselect_b32 s20, 1, s30\n"
"s_load_dwordx2 s[18:19], %[par], 160\n"
"v_add_u32 v3, s49, v0\n"
"v_add_u32 v4, s29, v3\n"
"v_add_u32 v3, s48, v3\n"
"ds_read_b128 v[64:67], v4 offset:19456\n"
"ds_read_b128 v[68:71], v4 offset:20480\n"
"ds_read_b128 v[72:75], v4 offset:21504\n"
"ds_read_b128 v[76:79], v4 offset:22528\n"
"ds_read_b128 v[80:83], v4 offset:23552\n"
"ds_read_b128 v[84:87], v4 offset:24576\n"
"ds_read_b128 v[88:91], v4 offset:25600\n"
"ds_read_b128 v[92:95], v4 offset:26624\n"
"ds_read_b128 v[24:27], v3\n"
"s_xor_b32 s49, s49, 12288\n"
"v_mov_b64 v[112:113], 0\n"
"v_mov_b64 v[114:115], 0\n"
"v_mov_b64 v[116:117], 0\n"
"v_mov_b64 v[118:119], 0\n"
"v_mov_b64 v[120:121], 0\n"
"v_mov_b64 v[122:123], 0\n"
"v_mov_b64 v[124:125], 0\n"
"v_mov_b64 v[126:127], 0\n"
"v_mov_b64 v[96:97], -1\n"
"v_mov_b64 v[98:99], -1\n"
"s_lshl_b32 s44, s41, 18\n"
"s_add_u32 s44, s44, s72\n"
"v_add_u32 v100, s44, v10\n"
"s_cmp_gt_u32 s42, 1\n"
"s_cselect_b32 s44, 0x1000, 0\n"
"s_add_u32 s50, s50, s44\n"
"s_addc_u32 s51, s51, 0\n"
"s_mov_b64 s[98:99], 0\n"
"s_mov_b64 s[90:91], s[52:53]\n"
"v_mov_b32 v101, 0\n"
"s_cmp_lt_u32 s42, 61\n"
"s_cselect_b32 s44, 0x4000, 0\n"
"s_add_u32 s46, s46, s44\n"
"s_addc_u32 s47, s47, 0\n"
"s_xor_b32 s44, s49, 12288\n"
"s_add_u32 m0, s89, s44\n"
"s_cmp_eq_u32 s41, 0\n"
"s_cselect_b32 s44, 4, 0\n"
"s_add_u32 s44, s44, 0x7fffc\n"
"s_add_u32 s96, s78, s44\n"
"s_addc_u32 s97, s79, 0\n"
"s_cmp_eq_u32 s41, 1\n"
"s_cselect_b32 s96, s8, s96\n"
"s_cselect_b32 s97, s9, s97\n"
"s_lshl_b32 s44, s41, 11\n"
"s_add_u32 s78, s78, s44\n"
"s_addc_u32 s79, s79, 0\n"
"v_mov_b32 v4, v31\n"
"v_lshlrev_b32 v3, 10, v0\n"
"s_lshl_b32 s44, s40, 4\n"
"v_add_u32 v3, s44, v3\n"
"s_add_u32 s76, s80, 0x80000\n"
"s_addc_u32 s77, s81, 0\n"
"s_mov_b32 s31, 1\n"
"ds_read_b64 v[28:29], v30 offset:8\n"
"CH_PB:\n"
"s_waitcnt vmcnt(7)\n"
"v_mfma_f32_16x16x32_bf16 v[108:111], v[32:35], v[104:107], 0\n"
"v_max3_u32 v30, v32, v33, v34\n"
"v_mov_b32 v31, v35\n"
"s_waitcnt vmcnt(6)\n"
"v_mfma_f32_16x16x32_bf16 v[108:111], v[36:39], v[104:107], v[108:111]\n"
"v_max3_u32 v30, v36, v37, v30\n"
"v_max3_u32 v31, v38, v39, v31\n"
"s_waitcnt vmcnt(5)\n"
"v_mfma_f32_16x16x32_bf16 v[108:111], v[40:43], v[104:107], v[108:111]\n"
"v_max3_u32 v30, v40, v41, v30\n"
"v_max3_u32 v31, v42, v43, v31\n"
"s_waitcnt vmcnt(4)\n"
"v_mfma_f32_16x16x32_bf16 v[108:111], v[44:47], v[104:107], v[108:111]\n"
"v_max3_u32 v30, v44, v45, v30\n"
"v_max3_u32 v31, v46, v47, v31\n"
"s_waitcnt vmcnt(3)\n"
"v_mfma_f32_16x16x32_bf16 v[108:111], v[48:51], v[104:107], v[108:111]\n"
"v_max3_u32 v30, v48, v49, v30\n"
"v_max3_u32 v31, v50, v51, v31\n"
"s_waitcnt vmcnt(2)\n"
"v_mfma_f32_16x16x32_bf16 v[108:111], v[52:55], v[104:107], v[108:111]\n"
"v_max3_u32 v30, v52, v53, v30\n"
"v_max3_u32 v31, v54, v55, v31\n"
"s_waitcnt vmcnt(1)\n"
"s_memtime s[44:45]\n"
"v_mfma_f32_16x16x32_bf16 v[108:111], v[56:59], v[104:107], v[108:111]\n"
"v_max3_u32 v30, v56, v57, v30\n"
"v_max3_u32 v31, v58, v59, v31\n"
"s_waitcnt vmcnt(0)\n"
"v_mfma_f32_16x16x32_bf16 v[108:111], v[60:63], v[104:107], v[108:111]\n"
"v_max3_u32 v30, v60, v61, v30\n"
"v_max3_u32 v31, v62, v63, v31\n"
"v_max_u32 v30, v30, v31\n"
"v_cmp_eq_u32 vcc, -1, v30\n"
"s_nop 3\n"
"v_permlane32_swap_b32 v108, v110\n"
"v_permlane32_swap_b32 v109, v111\n"
"v_add_f32 v108, v108, v110\n"
"v_add_f32 v109, v109, v111\n"
"v_cvt_pk_bf16_f32 v108, v108, v109\n"
"v_min_u32 v108, -2, v108\n"
"v_mov_b32 v109, v108\n"
"s_nop 1\n"
"v_permlane32_swap_b32 v108, v109\n"
"s_cbranch_vccz CH_PBOK\n"
"s_cmp_lt_u32 s31, 2\n"
"s_cbranch_scc1 CH_PBQ\n"
"s_sleep 2\n"
"CH_PBQ:\n"
"global_load_dwordx4 v[32:35], v4, s[56:57] sc1\n"
"global_load_dwordx4 v[36:39], v4, s[58:59] sc1\n"
"global_load_dwordx4 v[40:43], v4, s[60:61] sc1\n"
"global_load_dwordx4 v[44:47], v4, s[62:63] sc1\n"
"global_load_dwordx4 v[48:51], v4, s[64:65] sc1\n"
"global_load_dwordx4 v[52:55], v4, s[66:67] sc1\n"
"global_load_dwordx4 v[56:59], v4, s[68:69] sc1\n"
"global_load_dwordx4 v[60:63], v4, s[70:71] sc1\n"
"s_waitcnt vmcnt(0)\n"
"s_add_u32 s31, s31, 1\n"
"s_cmp_lt_u32 s31, 0x8000\n"
"s_cbranch_scc1 CH_PB\n"
"s_mov_b32 s54, 2\n"
"s_branch CH_FIN\n"
"CH_PBOK:\n"
"global_store_dwordx2 v11, v[108:109], s[80:81] sc1\n"
"s_waitcnt lgkmcnt(0)\n"
"s_sub_u32 s44, s44, s26\n"
"s_andn2_b32 s44, s44, 1\n"
"v_mov_b32 v110, s44\n"
"v_readfirstlane_b32 s16, v29\n"
"v_readfirstlane_b32 s34, v28\n"
"s_cmp_eq_u32 s16, s42\n"
"s_cbranch_scc0 CH_RW\n"
"CH_RW2:\n"
"s_and_b32 s82, s34, 0xffff\n"
"s_lshr_b32 s34, s34, 16\n"
"s_add_u32 s27, s27, s34\n"
"s_mov_b32 s21, s44\n"
"s_mov_b32 s22, 15\n"
"v_mov_b64 v[28:29], 0\n"
"v_mov_b64 v[30:31], 0\n"
"s_mov_b32 s44, s55\n"
"s_mov_b32 s55, s72\n"
"s_mov_b32 s72, s73\n"
"s_mov_b32 s73, s44\n"
"CH_NEXT:\n"
"s_sub_u32 s86, s26, s86\n"
"s_mul_i32 s86, s86, 3\n"
"s_ashr_i32 s86, s86, 3\n"
"s_max_i32 s86, s86, -400\n"
"s_min_i32 s86, s86, 100\n"
"s_add_u32 s34, s27, s86\n"
"s_add_u32 s87, s34, s26\n"
"CH_WC_A:\n"
"s_memtime s[44:45]\n"
"s_waitcnt lgkmcnt(0)\n"
"CH_WP_A:\n"
"s_sub_u32 s44, s87, s44\n"
"s_sub_u32 s44, s44, 110\n"
"s_cmp_gt_u32 s44, 16384\n"
"s_cbranch_scc1 CH_WE_A\n"
"s_cmp_gt_u32 s44, 252\n"
"s_cbranch_scc1 CH_WS_A\n"
"s_and_b32 s45, s44, 0xfc\n"
"s_getpc_b64 s[4:5]\n"
"s_add_u32 s4, s4, 276\n"
"s_addc_u32 s5, s5, 0\n"
"s_sub_u32 s4, s4, s45\n"
"s_subb_u32 s5, s5, 0\n"
"s_setpc_b64 s[4:5]\n"
"s_nop 0\n"
"s_nop 0\n"
"s_nop 0\n"
"s_nop 0\n"
"s_nop 0\n"
"s_nop 0\n"
"s_nop 0\n"
"s_nop 0\n"
"s_nop 0\n"
"s_nop 0\n"
"s_nop 0\n"
"s_nop 0\n"
"s_nop 0\n"
"s_nop 0\n"
"s_nop 0\n"
"s_nop 0\n"
"s_nop 0\n"
"s_nop 0\n"
"s_nop 0\n"
"s_nop 0\n"
"s_nop 0\n"
"s_nop 0\n"
"s_nop 0\n"
"s_nop 0\n"
"s_nop 0\n"
"s_nop 0\n"
"s_nop 0\n"
"s_nop 0\n"
"s_nop 0\n"
"s_nop 0\n"
"s_nop 0\n"
"s_nop 0\n"
"s_nop 0\n"
"s_nop 0\n"
"s_nop 0\n"
"s_nop 0\n"
"s_nop 0\n"
"s_nop 0\n"
"s_nop 0\n"
"s_nop 0\n"
"s_nop 0\n"
"s_nop 0\n"
"s_nop 0\n"
"s_nop 0\n"
"s_nop 0\n"
"s_nop 0\n"
"s_nop 0\n"
"s_nop 0\n"
"s_nop 0\n"
"s_nop 0\n"
"s_nop 0\n"
"s_nop 0\n"
"s_nop 0\n"
"s_nop 0\n"
"s_nop 0\n"
"s_nop 0\n"
"s_nop 0\n"
"s_nop 0\n"
"s_nop 0\n"
"s_nop 0\n"
"s_nop 0\n"
"s_nop 0\n"
"s_nop 0\n"
"CH_WE_A:\n"
"s_cmp_lt_u32 s42, 63\n"
"s_cbranch_scc1 CH_STEP\n"
"s_mov_b32 s30, 0\n"
"CH_PA_f:\n"
"global_load_dwordx4 v[32:35], v0, s[78:79] sc1\n"
"global_load_dwordx4 v[36:39], v0, s[78:79] offset:1024 sc1\n"
"s_cmp_eq_u32 s22, 0\n"
"s_cbranch_scc1 CH_PT_f\n"
"s_add_u32 s44, s21, s85\n"
"s_sub_u32 s44, s44, s27\n"
"s_cmp_gt_u32 s31, 1\n"
"s_cselect_b32 s44, -1, s44\n"
"s_cselect_b32 s16, s18, s19\n"
"s_cmp_gt_u32 s20, 1\n"
"s_cselect_b32 s44, 1, s44\n"
"s_cmp_gt_i32 s44, 0\n"
"s_cselect_b32 s17, s18, s19\n"
"s_cmp_lt_u32 s42, 4\n"
"s_cselect_b32 s16, 0, s16\n"
"s_cselect_b32 s17, 0, s17\n"
"s_and_b32 s44, s10, 0xffff\n"
"s_add_i32 s44, s44, s16\n"
"s_max_i32 s44, s44, 0\n"
"s_min_i32 s44, s44, 800\n"
"s_lshr_b32 s45, s10, 16\n"
"s_add_i32 s45, s45, s17\n"
"s_max_i32 s45, s45, 0\n"
"s_min_i32 s45, s45, 800\n"
"s_lshl_b32 s45, s45, 16\n"
"s_or_b32 s10, s44, s45\n"
"s_mul_i32 s44, s83, 0x3000a\n"
"s_lshr_b32 s44, s44, 4\n"
"s_and_b32 s44, s44, 0x0fff0fff\n"
"s_add_u32 s44, s44, s10\n"
"v_mov_b32 v111, s44\n"
"CH_PT_f:\n"
"s_mov_b32 exec_lo, s22\n"
"s_mov_b32 exec_hi, 0\n"
"global_store_dwordx2 v3, v[110:111], s[76:77] sc1\n"
"s_mov_b64 exec, -1\n"
"s_mov_b32 s22, 0\n"
"CH_PH_f:\n"
"s_add_u32 s30, s30, 1\n"
"s_waitcnt vmcnt(2)\n"
"ds_write_b128 v1, v[32:35]\n"
"v_max3_u32 v4, v32, v33, v34\n"
"s_waitcnt vmcnt(1)\n"
"ds_write_b128 v1, v[36:39] offset:1024\n"
"s_mov_b32 s7, 0x7fffffff\n"
"v_smfmac_f32_16x16x64_bf16 v[120:123], v[32:35], a[128:135], v9\n"
"v_max3_u32 v3, v35, v36, v37\n"
"v_smfmac_f32_16x16x64_bf16 v[124:127], v[32:35], a[192:199], v9\n"
"v_max3_u32 v4, v38, v39, v4\n"
"v_max_u32 v4, v3, v4\n"
"v_cmp_eq_u32 vcc, -1, v4\n"
"v_smfmac_f32_16x16x64_bf16 v[120:123], v[36:39], a[136:143], v9\n"
"v_xor_b32 v4, 0x800, v1\n"
"s_cbranch_vccnz CH_PF_f\n"
"s_waitcnt lgkmcnt(0)\n"
"s_barrier\n"
"ds_read_b128 v[40:43], v4\n"
"ds_read_b128 v[44:47], v4 offset:1024\n"
"v_xor_b32 v3, 0x1000, v1\n"
"v_smfmac_f32_16x16x64_bf16 v[124:127], v[36:39], a[200:207], v9\n"
"ds_read_b128 v[48:51], v3\n"
"ds_read_b128 v[52:55], v3 offset:1024\n"
"v_xor_b32 v4, 0x1800, v1\n"
"ds_read_b128 v[56:59], v4\n"
"ds_read_b128 v[60:63], v4 offset:1024\n"
"v_min_u32 v12, -2, v12\n"
"v_min_u32 v13, -2, v13\n"
"v_min_u32 v14, -2, v14\n"
"global_store_dwordx4 v0, v[12:15], s[50:51] sc1\n"
"s_or_b64 s[44:45], 62, %[tag]\n"
"v_mov_b32 v72, s44\n"
"v_mov_b32 v73, s45\n"
"s_cmp_eq_u32 s41, 2\n"
"s_cselect_b32 exec_lo, 1, 0\n"
"s_mov_b32 exec_hi, 0\n"
"global_store_dwordx2 v21, v[72:73], s[52:53] sc1\n"
"s_mov_b64 exec, -1\n"
"s_add_u32 s50, s50, 0x1000\n"
"s_addc_u32 s51, s51, 0\n"
"v_smfmac_f32_16x16x64_bf16 v[28:31], v[32:35], v[128:135], v9\n"
"v_smfmac_f32_16x16x64_bf16 v[28:31], v[36:39], v[136:143], v9\n"
"s_waitcnt lgkmcnt(5)\n"
"v_smfmac_f32_16x16x64_bf16 v[120:123], v[40:43], a[144:151], v9\n"
"v_smfmac_f32_16x16x64_bf16 v[124:127], v[40:43], a[208:215], v9\n"
"v_smfmac_f32_16x16x64_bf16 v[28:31], v[40:43], v[144:151], v9\n"
"s_waitcnt lgkmcnt(4)\n"
"v_smfmac_f32_16x16x64_bf16 v[120:123], v[44:47], a[152:159], v9\n"
"v_smfmac_f32_16x16x64_bf16 v[124:127], v[44:47], a[216:223], v9\n"
"v_smfmac_f32_16x16x64_bf16 v[28:31], v[44:47], v[152:159], v9\n"
"s_waitcnt lgkmcnt(3)\n"
"v_smfmac_f32_16x16x64_bf16 v[120:123], v[48:51], a[160:167], v9\n"
"v_smfmac_f32_16x16x64_bf16 v[124:127], v[48:51], a[224:231], v9\n"
"v_smfmac_f32_16x16x64_bf16 v[28:31], v[48:51], v[160:167], v9\n"
"s_waitcnt lgkmcnt(2)\n"
"v_smfmac_f32_16x16x64_bf16 v[120:123], v[52:55], a[168:175], v9\n"
"v_smfmac_f32_16x16x64_bf16 v[124:127], v[52:55], a[232:239], v9\n"
"v_smfmac_f32_16x16x64_bf16 v[28:31], v[52:55], v[168:175], v9\n"
"s_waitcnt lgkmcnt(1)\n"
"v_smfmac_f32_16x16x64_bf16 v[120:123], v[56:59], a[176:183], v9\n"
"v_smfmac_f32_16x16x64_bf16 v[124:127], v[56:59], a[240:247], v9\n"
"v_smfmac_f32_16x16x64_bf16 v[28:31], v[56:59], v[176:183], v9\n"
"s_waitcnt lgkmcnt(0)\n"
"v_smfmac_f32_16x16x64_bf16 v[120:123], v[60:63], a[184:191], v9\n"
"v_smfmac_f32_16x16x64_bf16 v[124:127], v[60:63], a[248:255], v9\n"
"v_smfmac_f32_16x16x64_bf16 v[28:31], v[60:63], v[184:191], v9\n"
"s_nop 5\n"
"v_add_f32 v120, v120, v122\n"
"v_add_f32 v121, v121, v123\n"
"v_add_f32 v124, v124, v126\n"
"v_add_f32 v125, v125, v127\n"
"v_add_f32 v28, v28, v30\n"
"v_add_f32 v29, v29, v31\n"
"v_cvt_pk_bf16_f32 v12, v120, v121\n"
"v_cvt_pk_bf16_f32 v13, v124, v125\n"
"v_cvt_pk_bf16_f32 v14, v28, v29\n"
"v_min_u32 v12, -2, v12\n"
"v_min_u32 v13, -2, v13\n"
"v_min_u32 v14, -2, v14\n"
"global_store_dwordx4 v0, v[12:15], s[50:51] sc1\n"
"s_branch CH_FIN\n"
"CH_SLOW:\n"
"s_mov_b64 exec, -1\n"
"v_max3_f32 v88, v112, v113, v114\n"
"v_max3_f32 v89, v115, v116, v117\n"
"v_max3_f32 v88, v118, v119, v88\n"
"v_max3_f32 v89, v120, v121, v89\n"
"v_max_f32 v88, v88, v89\n"
"v_mov_b32 v89, v88\n"
"s_nop 1\n"
"v_permlane16_swap_b32 v88, v89\n"
"v_max_f32 v88, v88, v89\n"
"v_mov_b32 v89, v88\n"
"s_nop 1\n"
"v_permlane32_swap_b32 v88, v89\n"
"v_max_f32 v64, v88, v89\n"
"v_sub_f32 v88, v112, v64\n"
"v_exp_f32 v88, v88\n"
"v_sub_f32 v89, v113, v64\n"
"v_exp_f32 v89, v89\n"
"v_sub_f32 v90, v114, v64\n"
"v_exp_f32 v90, v90\n"
"v_sub_f32 v91, v115, v64\n"
"v_exp_f32 v91, v91\n"
"v_sub_f32 v92, v116, v64\n"
"v_exp_f32 v92, v92\n"
"v_sub_f32 v93, v117, v64\n"
"v_exp_f32 v93, v93\n"
"v_sub_f32 v94, v118, v64\n"
"v_exp_f32 v94, v94\n"
"v_sub_f32 v95, v119, v64\n"
"v_exp_f32 v95, v95\n"
"v_cvt_pk_bf16_f32 v84, v88, v89\n"
"v_cvt_pk_bf16_f32 v85, v90, v91\n"
"v_cvt_pk_bf16_f32 v86, v92, v93\n"
"v_cvt_pk_bf16_f32 v87, v94, v95\n"
"v_sub_f32 v90, v120, v64\n"
"v_sub_f32 v91, v121, v64\n"
"v_exp_f32 v90, v90\n"
"v_exp_f32 v91, v91\n"
"s_nop 0\n"
"v_cvt_pk_bf16_f32 v20, v90, v91\n"
"v_mfma_f32_16x16x32_bf16 v[80:83], v[16:19], v[84:87], 0\n"
"v_mfma_f32_16x16x32_bf16 v[124:127], v[24:27], v[84:87], 0\n"
"v_mfma_f32_16x16x32_bf16 v[80:83], v[16:19], v[20:23], v[80:83]\n"
"v_mfma_f32_16x16x32_bf16 v[124:127], v[14:17], v[20:23], v[124:127]\n"
"s_mov_b32 exec_lo, 0xcccccccc\n"
"s_mov_b32 exec_hi, 0xcccccccc\n"
"s_mov_b64 vcc, 0\n"
"s_nop 7\n"
"s_branch CH_JOIN\n"
"CH_PF_m:\n"
"s_sleep 2\n"
"v_mov_b32 v3, 0\n"
"v_mov_b64 v[112:113], 0\n"
"v_mov_b64 v[114:115], 0\n"
"v_mov_b64 v[116:117], 0\n"
"v_mov_b64 v[118:119], 0\n"
"v_mov_b64 v[120:121], 0\n"
"v_mov_b64 v[122:123], 0\n"
"v_mov_b64 v[124:125], 0\n"
"v_mov_b64 v[126:127], 0\n"
"s_cmp_lt_u32 s30, 0x8000\n"
"s_cbranch_scc1 CH_PA_m\n"
"s_mov_b32 s54, 1\n"
"s_branch CH_FIN\n"
"CH_PF_f:\n"
"s_sleep 2\n"
"v_mov_b32 v3, 0\n"
"v_mov_b64 v[112:113], 0\n"
"v_mov_b64 v[114:115], 0\n"
"v_mov_b64 v[116:117], 0\n"
"v_mov_b64 v[118:119], 0\n"
"v_mov_b64 v[120:121], 0\n"
"v_mov_b64 v[122:123], 0\n"
"v_mov_b64 v[124:125], 0\n"
"v_mov_b64 v[126:127], 0\n"
"s_cmp_lt_u32 s30, 0x8000\n"
"s_cbranch_scc1 CH_PA_f\n"
"s_mov_b32 s54, 1\n"
"s_branch CH_FIN\n"
"CH_WS_G:\n"
"s_cmp_lt_u32 s44, 520\n"
"s_cbranch_scc1 CH_WC_G\n"
"s_sleep 3\n"
"s_branch CH_WC_G\n"
"CH_WS_A:\n"
"s_cmp_lt_u32 s44, 520\n"
"s_cbranch_scc1 CH_WC_A\n"
"s_sleep 3\n"
"s_branch CH_WC_A\n"
"CH_RW:\n"
"v_mov_b32 v30, 17536\n"
"s_mov_b32 s45, 0\n"
"CH_RW1:\n"
"ds_read_b64 v[28:29], v30 offset:8\n"
"s_add_u32 s45, s45, 1\n"
"s_waitcnt lgkmcnt(0)\n"
"v_readfirstlane_b32 s16, v29\n"
"v_readfirstlane_b32 s34, v28\n"
"s_cmp_eq_u32 s16, s42\n"
"s_cbranch_scc1 CH_RW2\n"
"s_cmp_lt_u32 s45, 0x10000\n"
"s_cbranch_scc1 CH_RW1\n"
"s_mov_b32 s34, 0x04b00c80\n"
"s_branch CH_RW2\n"
"CH_TSBAD:\n"
"s_mov_b32 s97, s39\n"
"s_mov_b32 s87, 0\n"
"v_cmp_eq_u32 vcc, -1, v3\n"
"s_cbranch_vccz CH_TSB2\n"
"CH_TSB1:\n"
"s_sleep 4\n"
"global_load_dwordx2 v[2:3], v0, s[96:97] sc1\n"
"s_add_u32 s87, s87, 1\n"
"s_waitcnt vmcnt(0)\n"
"v_cmp_eq_u32 vcc, -1, v3\n"
"s_cbranch_vccz CH_TSB2\n"
"s_cmp_lt_u32 s87, 40\n"
"s_cbranch_scc1 CH_TSB1\n"
"s_mov_b32 s34, 0\n"
"s_branch CH_TSB3\n"
"CH_TSB2:\n"
"v_subrev_u32 v3, s27, v3\n"
"s_nop 1\n"
"v_max_i32 v3, v3, v3 row_ror:1 row_mask:0xf bank_mask:0xf\n"
"s_nop 1\n"
"v_max_i32 v3, v3, v3 row_ror:2 row_mask:0xf bank_mask:0xf\n"
"s_nop 1\n"
"v_max_i32 v3, v3, v3 row_ror:4 row_mask:0xf bank_mask:0xf\n"
"s_nop 1\n"
"v_max_i32 v3, v3, v3 row_ror:8 row_mask:0xf bank_mask:0xf\n"
"s_nop 1\n"
"v_max_i32 v3, v3, v3 row_bcast:15 row_mask:0xa bank_mask:0xf\n"
"s_nop 1\n"
"v_max_i32 v3, v3, v3 row_bcast:31 row_mask:0xc bank_mask:0xf\n"
"s_nop 0\n"
"v_readlane_b32 s34, v3, 63\n"
"CH_TSB3:\n"
"s_add_u32 s87, s34, s85\n"
"s_and_b32 s44, s84, 0xffff\n"
"s_add_u32 s44, s34, s44\n"
"s_lshr_b32 s45, s84, 16\n"
"s_cmp_lt_u32 s42, 2\n"
"s_cselect_b32 s45, s44, s45\n"
"s_cmp_le_i32 s34, 280\n"
"s_cselect_b32 s34, s45, s44\n"
"s_cmp_lt_u32 s42, 2\n"
"s_cselect_b32 s87, 1, s87\n"
"s_cmp_gt_i32 s87, 0\n"
"s_cselect_b32 s34, s34, 0\n"
"s_add_u32 s44, s27, s34\n"
"s_add_u32 s87, s44, s82\n"
"s_add_u32 s87, s87, s26\n"
"s_sub_u32 s87, s87, s28\n"
"s_mov_b32 s97, 1\n"
"s_branch CH_WC_G\n"
"CH_FIN:\n"
"s_waitcnt vmcnt(0) lgkmcnt(0)\n"
        :: [par] "s"(par), [ids] "s"(ids), [tag] "s"(tg)
        : "v0", "v1", "v2", "v3", "v4", "v5", "v6", "v7", "v8", "v9", "v10", "v11", "v12", "v13", "v14", "v15", "v16", "v17", "v18", "v19", "v20", "v21", "v22", "v23", "v24", "v25", "v26", "v27", "v28", "v29", "v30", "v31", "v32", "v33", "v34", "v35", "v36", "v37", "v38", "v39", "v40", "v41", "v42", "v43", "v44", "v45", "v46", "v47", "v48", "v49", "v50", "v51", "v52", "v53", "v54", "v55", "v56", "v57", "v58", "v59", "v60", "v61", "v62", "v63", "v64", "v65", "v66", "v67", "v68", "v69", "v70", "v71", "v72", "v73", "v74", "v75", "v76", "v77", "v78", "v79", "v80", "v81", "v82", "v83", "v84", "v85", "v86", "v87", "v88", "v89", "v90", "v91", "v92", "v93", "v94", "v95", "v96", "v97", "v98", "v99", "v100", "v101", "v102", "v103", "v104", "v105", "v106", "v107", "v108", "v109", "v110", "v111", "v112", "v113", "v114", "v115", "v116", "v117", "v118", "v119", "v120", "v121", "v122", "v123", "v124", "v125", "v126", "v127", "v128", "v129", "v130", "v131", "v132", "v133", "v134", "v135", "v136", "v137", "v138", "v139", "v140", "v141", "v142", "v143", "v144", "v145", "v146", "v147", "v148", "v149", "v150", "v151", "v152", "v153", "v154", "v155", "v156", "v157", "v158", "v159", "v160", "v161", "v162", "v163", "v164", "v165", "v166", "v167", "v168", "v169", "v170", "v171", "v172", "v173", "v174", "v175", "v176", "v177", "v178", "v179", "v180", "v181", "v182", "v183", "v184", "v185", "v186", "v187", "v188", "v189", "v190", "v191", "v192", "v193", "v194", "v195", "v196", "v197", "v198", "v199", "v200", "v201", "v202", "v203", "v204", "v205", "v206", "v207", "v208", "v209", "v210", "v211", "v212", "v213", "v214", "v215", "v216", "v217", "v218", "v219", "v220", "v221", "v222", "v223", "v224", "v225", "v226", "v227", "v228", "v229", "v230", "v231", "v232", "v233", "v234", "v235", "v236", "v237", "v238", "v239", "v240", "v241", "v242", "v243", "v244", "v245", "v246", "v247", "v248", "v249", "v250", "v251", "v252", "v253", "v254", "v255", "a0", "a1", "a2", "a3", "a4", "a5", "a6", "a7", "a8", "a9", "a10", "a11", "a12", "a13", "a14", "a15", "a16", "a17", "a18", "a19", "a20", "a21", "a22", "a23", "a24", "a25", "a26", "a27", "a28", "a29", "a30", "a31", "a32", "a33", "a34", "a35", "a36", "a37", "a38", "a39", "a40", "a41", "a42", "a43", "a44", "a45", "a46", "a47", "a48", "a49", "a50", "a51", "a52", "a53", "a54", "a55", "a56", "a57", "a58", "a59", "a60", "a61", "a62", "a63", "a64", "a65", "a66", "a67", "a68", "a69", "a70", "a71", "a72", "a73", "a74", "a75", "a76", "a77", "a78", "a79", "a80", "a81", "a82", "a83", "a84", "a85", "a86", "a87", "a88", "a89", "a90", "a91", "a92", "a93", "a94", "a95", "a96", "a97", "a98", "a99", "a100", "a101", "a102", "a103", "a104", "a105", "a106", "a107", "a108", "a109", "a110", "a111", "a112", "a113", "a114", "a115", "a116", "a117", "a118", "a119", "a120", "a121", "a122", "a123", "a124", "a125", "a126", "a127", "a128", "a129", "a130", "a131", "a132", "a133", "a134", "a135", "a136", "a137", "a138", "a139", "a140", "a141", "a142", "a143", "a144", "a145", "a146", "a147", "a148", "a149", "a150", "a151", "a152", "a153", "a154", "a155", "a156", "a157", "a158", "a159", "a160", "a161", "a162", "a163", "a164", "a165", "a166", "a167", "a168", "a169", "a170", "a171", "a172", "a173", "a174", "a175", "a176", "a177", "a178", "a179", "a180", "a181", "a182", "a183", "a184", "a185", "a186", "a187", "a188", "a189", "a190", "a191", "a192", "a193", "a194", "a195", "a196", "a197", "a198", "a199", "a200", "a201", "a202", "a203", "a204", "a205", "a206", "a207", "a208", "a209", "a210", "a211", "a212", "a213", "a214", "a215", "a216", "a217", "a218", "a219", "a220", "a221", "a222", "a223", "a224", "a225", "a226", "a227", "a228", "a229", "a230", "a231", "a232", "a233", "a234", "a235", "a236", "a237", "a238", "a239", "a240", "a241", "a242", "a243", "a244", "a245", "a246", "a247", "a248", "a249", "a250", "a251", "a252", "a253", "a254", "a255", "s4", "s5", "s6", "s7", "s8", "s9", "s10", "s11", "s12", "s13", "s14", "s15", "s16", "s17", "s18", "s19", "s20", "s21", "s22", "s23", "s24", "s25", "s26", "s27", "s28", "s29", "s30", "s31", "s34", "s35", "s36", "s37", "s38", "s39", "s40", "s41", "s42", "s43", "s44", "s45", "s46", "s47", "s48", "s49", "s50", "s51", "s52", "s53", "s54", "s55", "s56", "s57", "s58", "s59", "s60", "s61", "s62", "s63", "s64", "s65", "s66", "s67", "s68", "s69", "s70", "s71", "s72", "s73", "s74", "s75", "s76", "s77", "s78", "s79", "s80", "s81", "s82", "s83", "s84", "s85", "s86", "s87", "s88", "s89", "s90", "s91", "s92", "s93", "s94", "s95", "s96", "s97", "s98", "s99", "vcc", "scc", "memory");
        return;
    }
    if (x < 4) {
        const int j = (r - 16) * 4 + x;
        if (p.phases & 1) { if (j >= 62 && wave == 0) probe_page(p, tg, j - 62, lane); phase_ln(p, tg, j, wave, lane); poison(p, tg, j, wave, lane); }
        if (j == 50 && wave == 0 && !(p.pad & 1)) {
            char* fl = (char*)p.ws + WS_FLAGS;
            unsigned dummy;
            for (unsigned s = 0; s < SPIN_CAP && !census(fl, lane, tg, 0, dummy); ++s) __builtin_amdgcn_s_sleep(32);
            setflag(fl + F_EPOCH, tg);
        }
        if ((p.phases & 8) && j < 48) {
            const int u = j * 4 + wave;
            doze_until<127>((char*)p.prog + (u & 31) * 512, 56, tg);
            attn_tail(p, tg, u & 31, 61 + (u >> 6), (u >> 5) & 1, wave, lane);
        }
    } else {
        if (r < 8) {
            if (p.phases & 2) phase_proj<0>(p, tg, r * 4 + (x - 4), 0, 5, 1, wave, lane);
        } else if (r < 16) {
            const int j = (r - 8) * 4 + (x - 4);
            for (int i = 0; i < (j < 16 ? QF_DELAY : FF_DELAY); ++i) __builtin_amdgcn_s_sleep(127);
            if (j < 16) { if (p.phases & 4) phase_proj<2>(p, tg, j, 0, 1, 1, wave, lane); }
            else if (j < 27) { if (p.phases & 4) phase_proj<1>(p, tg, j - 16, 0, 1, 1, wave, lane); }
        } else if (p.phases & 2) phase_proj<0>(p, tg, (r & 7) * 4 + (x - 4), 1 + ((r - 16) >> 3), 3 + ((r - 16) >> 3), 64, wave, lane);
        if (r < 28) { if (p.phases & 32) phase_out(p, tg, r < 8 ? 51 - (r >> 2) : r < 16 ? 63 - (r >> 2) : (r - 16) >> 2, r < 16, x - 4, r & 3, wave, lane); }
        else if (p.phases & 8) { const int u = ((r - 28) * 4 + (x - 4)) * 4 + wave; phase_attn(p, tg, u & 31, u >> 5, 60, 2, wave, lane); }
    }
}

#define TUNE_W 2800
#define TUNE_X 1115
#define TUNE_RB (1100 | 800 << 16)
#define TUNE_LA 280
#define TUNE_CB 60
#define TUNE_CA -2
#define STAG 60
static void launch_phases(void* const* d_in, void* d_out, void* d_ws, hipStream_t stream, int phases, int W, int X, int RB, int LA, int CB, int CA) {
    char* ws = (char*)(((uintptr_t)d_ws + 2 * MB1 - 1) & ~(uintptr_t)(2 * MB1 - 1));
    Par p;
    p.wq = d_in[2]; p.wk = d_in[3]; p.wv = d_in[4]; p.wo = d_in[5]; p.mem0 = d_in[1];
    p.kv = ws + WS_KV; p.kvf = ws + WS_FLAGS + F_KV; p.kvm = ws + WS_KVM; p.prog = ws + WS_FLAGS + F_PROG; p.ws = ws; p.par = ws + WS_FLAGS + F_PAR; p.dbg = ws + WS_DBG;
    p.W = W; p.PI = W + X; p.RB = RB; p.LA = LA;
    p.x = d_in[0]; p.wg = d_in[8]; p.wu = d_in[9]; p.wd = d_in[10]; p.out = d_out; p.phases = phases; p.pad = (phases != 63) | STAG << 8; p.CB = CB; p.CA = CA;
    hipLaunchKernelGGL(rmt_kernel, dim3(256), dim3(256), LDS_BYTES, stream, p);
}

extern "C" void kernel_launch(void* const* d_in, const int* in_sizes, int n_in, void* d_out, int out_size, void* d_ws, size_t ws_size, hipStream_t stream) {
    (void)in_sizes; (void)n_in; (void)out_size; (void)ws_size;
    (void)hipFuncSetAttribute((const void*)rmt_kernel, hipFuncAttributeMaxDynamicSharedMemorySize, LDS_BYTES);
    launch_phases(d_in, d_out, d_ws, stream, 63, TUNE_W, TUNE_X, TUNE_RB, TUNE_LA, TUNE_CB, TUNE_CA);
}
```
